```python
import torch

def _softmax(x, axis=-1):
    return torch.softmax(x, dim=axis)
def _gelu(x):
    return torch.nn.functional.gelu(x, approximate="tanh")
def _silu(x):
    return torch.nn.functional.silu(x)
def _sigmoid(x):
    return torch.sigmoid(x)
def _layernorm(x, axis=-1, eps=1e-5):
    m = x.mean(dim=axis, keepdim=True)
    v = x.var(dim=axis, keepdim=True, unbiased=False)
    return (x - m) / torch.sqrt(v + eps)
def _scatter_add(tgt, idx, src, axis=0):
    return tgt.clone().index_add_(axis, idx, src)
def _segment_sum(src, seg, n):
    out = torch.zeros((n,) + src.shape[1:], dtype=src.dtype)
    return out.index_add_(0, seg, src)
def _pad(x, pw):
    flat = tuple(v for pair in reversed(pw) for v in pair)
    return torch.nn.functional.pad(x, flat)

kernel_name = 'gated_mix__ssm_conv_h3__ssm_conv_s4__prompt'

def setup_inputs():

    _w = (lambda a: (lambda t: t.to(torch.bfloat16) if t.is_floating_point() else t)(torch.as_tensor(a)))
    def _A_setup():
        B, S, D = 1, 2048, 512
        ks = 4
        return {
            "q": _w((lambda *s: torch.randn(*s, dtype=torch.bfloat16) * 0.05)(B, S, D)),
            "k": _w((lambda *s: torch.randn(*s, dtype=torch.bfloat16) * 0.4)(B, S, D)),
            "v": _w((lambda *s: torch.randn(*s, dtype=torch.bfloat16) * 0.2)(B, S, D)),
            "shift_k": _w((lambda *s: torch.randn(*s, dtype=torch.bfloat16) * 0.4)(D, ks) * 0.5),
            "ssm_k": _w((lambda *s: torch.randn(*s, dtype=torch.bfloat16) * 0.4)(D, S) * (1.0 / S ** 0.5)),
            "ssm_D": _w((lambda *s: torch.randn(*s, dtype=torch.bfloat16) * 0.2)(D)),
        }
    _A_in = _A_setup()

    def _B_setup():
        B, L, D, N = 1, 2048, 512, 128
        A_re = -(lambda _x: torch.exp(torch.as_tensor(_x)))((lambda *s: torch.randn(*s, dtype=torch.bfloat16) * 3.2)(D, N))
        A_im = torch.pi * (lambda *s: torch.randn(*s, dtype=torch.bfloat16) * 0.05)(D, N)
        B_re = (lambda *s: torch.randn(*s, dtype=torch.bfloat16) * 0.1)(D, N)
        B_im = (lambda *s: torch.randn(*s, dtype=torch.bfloat16) * 0.1)(D, N)
        C_re = (lambda *s: torch.randn(*s, dtype=torch.bfloat16) * 0.1)(D, N)
        C_im = (lambda *s: torch.randn(*s, dtype=torch.bfloat16) * 0.1)(D, N)
        log_dt = (lambda *s: torch.randn(*s, dtype=torch.bfloat16) * 0.05)(D) * 0.5 - 4.0
        Dskip = (lambda *s: torch.randn(*s, dtype=torch.bfloat16) * 0.05)(D)
        return {
            "u": _w((lambda *s: torch.randn(*s, dtype=torch.bfloat16) * 0.05)(B, L, D)),
            "A_re": _w(A_re),
            "A_im": _w(A_im),
            "B_re": _w(B_re),
            "B_im": _w(B_im),
            "C_re": _w(C_re),
            "C_im": _w(C_im),
            "log_dt": _w(log_dt),
            "Dskip": _w(Dskip),
        }
    _B_in = _B_setup()

    return {
        "A_q": _A_in["q"],
        "A_k": _A_in["k"],
        "A_v": _A_in["v"],
        "A_shift_k": _A_in["shift_k"],
        "A_ssm_k": _A_in["ssm_k"],
        "A_ssm_D": _A_in["ssm_D"],
        "B_u": _B_in["u"],
        "B_A_re": _B_in["A_re"],
        "B_A_im": _B_in["A_im"],
        "B_B_re": _B_in["B_re"],
        "B_B_im": _B_in["B_im"],
        "B_C_re": _B_in["C_re"],
        "B_C_im": _B_in["C_im"],
        "B_log_dt": _B_in["log_dt"],
        "B_Dskip": _B_in["Dskip"],
        "gate_w": _w((lambda *s: torch.randn(*s, dtype=torch.bfloat16) * 0.05)(512) * 16.0),
    }

def reference(A_q, A_k, A_v, A_shift_k, A_ssm_k, A_ssm_D, B_u, B_A_re, B_A_im, B_B_re, B_B_im, B_C_re, B_C_im, B_log_dt, B_Dskip, gate_w):
    def _A_ref(q, k, v, shift_k, ssm_k, ssm_D):
        B, S, D = q.shape
        ks = shift_k.shape[-1]
        k_pad = _pad(k, ((0, 0), (ks - 1, 0), (0, 0)))
        k_shift = torch.zeros_like(k)
        for t in range(ks):
            k_shift = k_shift + k_pad[:, t:t + S, :] * shift_k[:, ks - 1 - t][None, None, :]
        kv = k_shift * v
        L = 2 * S
        kv_pad = _pad(kv, ((0, 0), (0, S), (0, 0)))
        kf_pad = _pad(ssm_k, ((0, 0), (0, S)))
        kv_f = (lambda x, axes=(-2,-1): torch.fft.rfft2(x.float(), dim=axes))(kv_pad[:, None, :, :], axes=(1, 2))
        kf_f = (lambda x, axes=(-2,-1): torch.fft.rfft2(x.float(), dim=axes))(kf_pad[:, None, :], axes=(1, 2))
        kf_f = (lambda x, *d: x.permute(*d))(kf_f, 1, 2, 0)
        y_f = kv_f * kf_f[None, :, :, :]
        y = (lambda x, s, axes=(-2,-1): torch.fft.irfft2(x, s=s, dim=axes).to(torch.bfloat16))(y_f, (1, L), axes=(1, 2))[:, 0, :S, :]
        y = y + kv * ssm_D[None, None, :]
        return q * y

    def _B_ref(u, A_re, A_im, B_re, B_im, C_re, C_im, log_dt, Dskip):
        Bsz, L, D = u.shape
        dt = torch.exp(log_dt)[:, None]
        A = A_re + 1j * A_im
        Bc = B_re + 1j * B_im
        Cc = C_re + 1j * C_im
        logdA = dt * A
        dB = dt * Bc
        l = (lambda x, dt: x.to(dt))(torch.arange(L), A_re.dtype)
        P = torch.exp(logdA[:, :, None] * l[None, None, :])
        K = torch.einsum("dn,dnl,dn->dl", Cc, P, dB).real
        L2 = 2 * L
        Kp = _pad(K, ((0, 0), (0, L)))
        up = (lambda x, *d: x.permute(*d))(u, 0, 2, 1)
        up = _pad(up, ((0, 0), (0, 0), (0, L)))
        Kf = (lambda x, axes=(-2,-1): torch.fft.rfft2(x.float(), dim=axes))(Kp[:, None, :], axes=(-2, -1))
        Uf = (lambda x, axes=(-2,-1): torch.fft.rfft2(x.float(), dim=axes))(up[:, :, None, :], axes=(-2, -1))
        Yf = Uf * Kf[None, :, :, :]
        y = (lambda x, s, axes=(-2,-1): torch.fft.irfft2(x, s=s, dim=axes).to(torch.bfloat16))(Yf, (1, L2), axes=(-2, -1))[:, :, 0, :L]
        y = (lambda x, *d: x.permute(*d))(y, 0, 2, 1) + u * Dskip[None, None, :]
        return y

    x = A_q
    g = _sigmoid((x * gate_w).sum(-1, keepdims=True))
    a = _A_ref(x, A_k, A_v, A_shift_k, A_ssm_k, A_ssm_D)
    b = _B_ref(x, B_A_re, B_A_im, B_B_re, B_B_im, B_C_re, B_C_im, B_log_dt, B_Dskip)
    return g * a + (1 - g) * b
```

```cpp
#include <hip/hip_runtime.h>
#include <hip/hip_bf16.h>
#include <stdint.h>

typedef unsigned short u16;
__device__ __forceinline__ float bf2f(u16 v) { return __uint_as_float(((unsigned)v) << 16); }
__device__ __forceinline__ u16 f2bf(float f) { return __builtin_bit_cast(u16, (__bf16)f); }
__device__ __forceinline__ float rb(float f) { return bf2f(f2bf(f)); }

constexpr int S = 2048, D = 512, N = 128;

__global__ void k_prep(const u16* q, const u16* k, const u16* v, const u16* sk, const u16* gw, u16* kv, float* g) {
    int s = blockIdx.x, d = threadIdx.x;
    float acc = 0.f;
    for (int t = 0; t < 4; ++t) {
        int ss = s + t - 3;
        float kk = ss >= 0 ? bf2f(k[ss * D + d]) : 0.f;
        float p = rb(kk * bf2f(sk[d * 4 + 3 - t]));
        acc = rb(acc + p);
    }
    kv[s * D + d] = f2bf(acc * bf2f(v[s * D + d]));
    __shared__ float red[512];
    red[d] = rb(bf2f(q[s * D + d]) * bf2f(gw[d]));
    __syncthreads();
    for (int o = 256; o > 0; o >>= 1) { if (d < o) red[d] += red[d + o]; __syncthreads(); }
    if (d == 0) { float z = rb(red[0]); g[s] = rb(1.f / (1.f + expf(-z))); }
}
__global__ void k_gen(const u16* are, const u16* aim, const u16* bre, const u16* bim, const u16* cre, const u16* cim, const u16* ldt, float* K) {
    int d = blockIdx.x;
    float dt = rb(expf(bf2f(ldt[d])));
    for (int l = threadIdx.x; l < S; l += blockDim.x) {
        float lf = rb((float)l);
        float acc = 0.f;
        for (int n = 0; n < N; ++n) {
            float a = dt * bf2f(are[d * N + n]), b = dt * bf2f(aim[d * N + n]);
            float br = dt * bf2f(bre[d * N + n]), bi = dt * bf2f(bim[d * N + n]);
            float cr = bf2f(cre[d * N + n]), ci = bf2f(cim[d * N + n]);
            float xr = cr * br - ci * bi, xi = cr * bi + ci * br;
            float e = expf(a * lf), sn, cs;
            sincosf(b * lf, &sn, &cs);
            acc += e * (xr * cs - xi * sn);
        }
        K[d * S + l] = acc;
    }
}
__global__ void k_conv(const u16* q, const u16* kv, const u16* ssm, const u16* sD, const float* K, const u16* Dsk, const float* g, u16* out) {
    int d = blockIdx.x;
    __shared__ float su[S], skv[S], sa[S], sb[S];
    for (int l = threadIdx.x; l < S; l += blockDim.x) { su[l] = bf2f(q[l * D + d]); skv[l] = bf2f(kv[l * D + d]); sa[l] = bf2f(ssm[d * S + l]); sb[l] = K[d * S + l]; }
    __syncthreads();
    for (int s = threadIdx.x; s < S; s += blockDim.x) {
        float ya = 0.f, yb = 0.f;
        for (int j = 0; j <= s; ++j) { ya += skv[s - j] * sa[j]; yb += su[s - j] * sb[j]; }
        ya = rb(ya); yb = rb(yb);
        float a = rb(su[s] * rb(ya + rb(skv[s] * bf2f(sD[d]))));
        float b = rb(yb + rb(su[s] * bf2f(Dsk[d])));
        float gg = g[s];
        out[s * D + d] = f2bf(rb(gg * a) + rb(rb(1.f - gg) * b));
    }
}

extern "C" void kernel_launch(void* const* d_in, const int* in_sizes, int n_in,
                              void* d_out, int out_size, void* d_ws, size_t ws_size,
                              hipStream_t stream) {
    const u16* q = (const u16*)d_in[0]; const u16* k = (const u16*)d_in[1]; const u16* v = (const u16*)d_in[2];
    const u16* sk = (const u16*)d_in[3]; const u16* ssm = (const u16*)d_in[4]; const u16* sD = (const u16*)d_in[5];
    const u16* are = (const u16*)d_in[7]; const u16* aim = (const u16*)d_in[8]; const u16* bre = (const u16*)d_in[9];
    const u16* bim = (const u16*)d_in[10]; const u16* cre = (const u16*)d_in[11]; const u16* cim = (const u16*)d_in[12];
    const u16* ldt = (const u16*)d_in[13]; const u16* Dsk = (const u16*)d_in[14]; const u16* gw = (const u16*)d_in[15];
    char* ws = (char*)d_ws;
    u16* kv = (u16*)ws; float* g = (float*)(ws + (4 << 20)); float* K = (float*)(ws + (8 << 20));
    k_prep<<<S, 512, 0, stream>>>(q, k, v, sk, gw, kv, g);
    k_gen<<<D, 256, 0, stream>>>(are, aim, bre, bim, cre, cim, ldt, K);
    k_conv<<<D, 256, 0, stream>>>(q, kv, ssm, sD, K, Dsk, g, (u16*)d_out);
}
```

```cpp
#include <hip/hip_runtime.h>
#include <stdint.h>

typedef unsigned short u16;
typedef unsigned int u32;
typedef unsigned long long u64;
typedef u32 u32x4 __attribute__((ext_vector_type(4)));
typedef u32 u32x2 __attribute__((ext_vector_type(2)));
typedef float f32x4 __attribute__((ext_vector_type(4)));
typedef float f32x2 __attribute__((ext_vector_type(2)));
typedef __bf16 bf16x2 __attribute__((ext_vector_type(2)));
typedef __bf16 bf16x8 __attribute__((ext_vector_type(8)));
#define GAS __attribute__((address_space(1)))
#define LAS __attribute__((address_space(3)))
typedef GAS u64 gu64;

#define FUSED 1
#define STAMPS 0

constexpr int S = 2048, D = 512, NS = 128;
constexpr int NWG = 256, NT = 512;

constexpr size_t WS_EPOCH = 0;
constexpr size_t WS_FLAGS = 4096;
constexpr size_t WS_G     = 16384;
constexpr size_t WS_SIG   = 65536;
constexpr size_t WS_STAMP = 8u << 20;

constexpr int KT_COPY = 4160;
constexpr int KT_CC   = 4 * KT_COPY;
constexpr int L_KT    = 0;
constexpr int SIG_ST  = 4608;
constexpr int L_SIG   = L_KT + 4 * KT_CC;
constexpr int L_T     = L_SIG + 4 * SIG_ST;
constexpr int L_H     = L_T + 3 * 8192;
constexpr int L_KP    = L_H + 1024;
constexpr int PART_ST = 2624;
constexpr int L_PART  = L_KP + 4096;
constexpr int L_KB    = L_PART + 8 * PART_ST;
constexpr int L_G     = L_KB + 2 * 1312;
constexpr int L_MISC  = L_G + 4096;
constexpr int L_END   = L_MISC + 64;

__device__ __forceinline__ u32 pk(float lo, float hi) { f32x2 v = {lo, hi}; bf16x2 b = __builtin_convertvector(v, bf16x2); return __builtin_bit_cast(u32, b); }
__device__ __forceinline__ float rb(float x) { return __uint_as_float(pk(0.f, x)); }
__device__ __forceinline__ float lo2f(u32 d) { return __uint_as_float(d << 16); }
__device__ __forceinline__ float hi2f(u32 d) { return __uint_as_float(d & 0xffff0000u); }
__device__ __forceinline__ float bf2f(u16 v) { return __uint_as_float(((u32)v) << 16); }

template <typename T> __device__ __forceinline__ T lds_ld(int off) { return *(const LAS T*)(size_t)(u32)off; }
template <typename T> __device__ __forceinline__ void lds_st(int off, T v) { *(LAS T*)(size_t)(u32)off = v; }

struct Params {
    const u16 *q, *k, *v, *sk, *ssm, *sD, *are, *aim, *bre, *bim, *cre, *cim, *ldt, *dsk, *gw;
    u16* out; char* ws;
};

__device__ __forceinline__ void stamp(char* ws, int slot) {
}

__device__ __forceinline__ u32x4 tr8(u32x4 d, u32 sel) {
    u32 e[4];
#pragma unroll
    for (int m = 0; m < 4; ++m) {
        u32 p = __builtin_amdgcn_update_dpp(0u, d[m], 0x128, 0xf, 0xf, false);
        e[m] = __builtin_amdgcn_perm(p, d[m], sel);
    }
    auto a = __builtin_amdgcn_permlane16_swap(e[0], e[1], false, false);
    auto b = __builtin_amdgcn_permlane16_swap(e[2], e[3], false, false);
    auto c = __builtin_amdgcn_permlane32_swap(a[0], b[0], false, false);
    auto f = __builtin_amdgcn_permlane32_swap(a[1], b[1], false, false);
    u32x4 o = {f[1], c[1], f[0], c[0]};
    return o;
}

__device__ __forceinline__ void write_copies(int base, int j, u32 d0, u32 d1, u32 d2, u32 d3, u32 d4, u32 d5) {
    u32 a10 = __builtin_amdgcn_alignbit(d1, d0, 16), a21 = __builtin_amdgcn_alignbit(d2, d1, 16), a32 = __builtin_amdgcn_alignbit(d3, d2, 16),
        a43 = __builtin_amdgcn_alignbit(d4, d3, 16), a54 = __builtin_amdgcn_alignbit(d5, d4, 16);
    int o = base + 32 + 16 * j;
    lds_st<u32x4>(o, (u32x4){d0, d1, d2, d3});
    lds_st<u32x4>(o + KT_COPY, (u32x4){a10, a21, a32, a43});
    lds_st<u32x4>(o + 2 * KT_COPY, (u32x4){d1, d2, d3, d4});
    lds_st<u32x4>(o + 3 * KT_COPY, (u32x4){a21, a32, a43, a54});
    if (j == 0) {
        const u32x4 z = {0, 0, 0, 0};
#pragma unroll
        for (int a = 0; a < 4; ++a) lds_st<u32x4>(base + a * KT_COPY, z);
        lds_st<u32x4>(base + 16, z);
        lds_st<u32x4>(base + 16 + KT_COPY, (u32x4){0, 0, 0, d0 << 16});
        lds_st<u32x4>(base + 16 + 2 * KT_COPY, (u32x4){0, 0, 0, d0});
        lds_st<u32x4>(base + 16 + 3 * KT_COPY, (u32x4){0, 0, d0 << 16, a10});
    }
}

__device__ __forceinline__ bool wait_flags(const gu64* f, u64 tag, bool active) {
    for (u32 spins = 0; spins < (1u << 18); ++spins) {
        u64 x = active ? __hip_atomic_load(f, __ATOMIC_RELAXED, __HIP_MEMORY_SCOPE_AGENT) : tag;
        if (__all(x == tag)) return true;
        __builtin_amdgcn_s_sleep(1);
    }
    return false;
}

template <int PHASE>
__global__ void __launch_bounds__(NT, 2) fused_kernel(Params P) {
    extern __shared__ __attribute__((aligned(16))) char smem_[];
    const int tid = threadIdx.x, lane = tid & 63;
    const int wv = __builtin_amdgcn_readfirstlane(tid >> 6);
    const int w = blockIdx.x, grp = w & 7, ti = w >> 3;
    const int d0 = 64 * grp + 2 * ti;
    char* ws = P.ws;
    stamp(ws, 0);
    u64 tag = 0;
    if (PHASE == 0) tag = *(const u64*)(ws + WS_EPOCH) + 1;

    const __amdgpu_buffer_rsrc_t sig_rsrc = __builtin_amdgcn_make_buffer_rsrc(ws + WS_SIG, 0, 2 * D * S * 2, 0x00020000);
    const __amdgpu_buffer_rsrc_t g_rsrc = __builtin_amdgcn_make_buffer_rsrc(ws + WS_G, 0, S * 2, 0x00020000);

    u32x4 lq, lk, lv, lh, gq, gwv; u32x2 skw;
    if (PHASE != 2) {
        const int r = lane >> 3, o = lane & 7;
        const size_t off = (size_t)(64 * ti + 8 * wv + r) * D + 64 * grp + 8 * o;
        lq = *(const u32x4*)(P.q + off);
        lk = *(const u32x4*)(P.k + off);
        lv = *(const u32x4*)(P.v + off);
        lh = (u32x4){0, 0, 0, 0};
        if (wv == 0 && ti > 0) lh = *(const u32x4*)(P.k + off - 8 * D);
        gq = *(const u32x4*)(P.q + (size_t)(8 * w + wv) * D + 8 * lane);
        gwv = *(const u32x4*)(P.gw + 8 * lane);
        skw = *(const u32x2*)(P.sk + (64 * grp + 8 * wv + (lane >> 3)) * 4);
    }
    u16 pa = 0, pb = 0, pc = 0, pd = 0, pe = 0, pf = 0, pl = 0;
    u32x4 sm; u32x2 sm2;
    if (PHASE != 1) {
        if (tid < 256) {
            const int idx = (d0 + (tid >> 7)) * NS + (tid & 127);
            pa = P.are[idx]; pb = P.aim[idx]; pc = P.bre[idx]; pd = P.bim[idx]; pe = P.cre[idx]; pf = P.cim[idx]; pl = P.ldt[d0 + (tid >> 7)];
        }
        const int ch = tid >> 8, j = tid & 255;
        const u16* src = P.ssm + (size_t)(d0 + ch) * S + 8 * j;
        sm = *(const u32x4*)src;
        sm2 = (u32x2){0, 0};
        if (j < 255) sm2 = *(const u32x2*)(src + 8);
    }

    if (PHASE != 2) {
        const int r = lane >> 3, o = lane & 7;
        const u32 sel = (r & 1) ? 0x07060302u : 0x01000504u;
        const int c = 8 * o + r;
        const int a = L_T + c * 128 + (((7 - wv) ^ o) << 4);
        lds_st<u32x4>(a, tr8(lq, sel));
        lds_st<u32x4>(a + 8192, tr8(lk, sel));
        lds_st<u32x4>(a + 16384, tr8(lv, sel));
        if (wv == 0) lds_st<u32x4>(L_H + c * 16, tr8(lh, sel));
        float z = 0.f;
#pragma unroll
        for (int m = 0; m < 4; ++m) { z += rb(lo2f(gq[m]) * lo2f(gwv[m])); z += rb(hi2f(gq[m]) * hi2f(gwv[m])); }
#pragma unroll
        for (int m = 32; m >= 1; m >>= 1) z += __shfl_xor(z, m);
        z = rb(z);
        const float g = 1.f / (1.f + expf(-z));
        if (lane == 0) lds_st<u16>(L_MISC + 2 * wv, (u16)(pk(g, g) & 0xffffu));
    }
    if (tid == 0) lds_st<u32>(L_MISC + 16, 0u);
    if (PHASE != 1) {
        if (tid < 256) {
            const float dt = rb(expf(bf2f(pl)));
            const float a = dt * bf2f(pa), b = dt * bf2f(pb), br = dt * bf2f(pc), bi = dt * bf2f(pd), cr = bf2f(pe), ci = bf2f(pf);
            f32x4 e = {a * 1.44269504088896341f, b * 0.15915494309189535f, cr * br - ci * bi, cr * bi + ci * br};
            lds_st<f32x4>(L_KP + tid * 16, e);
        }
        const int ch = tid >> 8, j = tid & 255;
        write_copies(L_KT + (2 * ch) * KT_CC, j, sm[0], sm[1], sm[2], sm[3], sm2[0], sm2[1]);
        if (tid < 4 * 30) lds_st<u32x4>(L_SIG + (tid / 30) * SIG_ST + 4096 + (tid % 30) * 16, (u32x4){0, 0, 0, 0});
    }
    stamp(ws, 1);
    __syncthreads();

    if (PHASE != 2) {
        const int cc = lane >> 3, oct = lane & 7, c = 8 * wv + cc;
        const int a = L_T + c * 128 + ((oct ^ wv) << 4);
        const u32x4 tq = lds_ld<u32x4>(a), tk = lds_ld<u32x4>(a + 8192), tv = lds_ld<u32x4>(a + 16384);
        const int an = oct < 7 ? (L_T + 8192 + c * 128 + (((oct + 1) ^ wv) << 4)) : (L_H + c * 16);
        const u32x2 tn = lds_ld<u32x2>(an);
        float kk[11];
#pragma unroll
        for (int m = 0; m < 4; ++m) { kk[2 * m] = lo2f(tk[m]); kk[2 * m + 1] = hi2f(tk[m]); }
        kk[8] = lo2f(tn[0]); kk[9] = hi2f(tn[0]); kk[10] = lo2f(tn[1]);
        const float s0 = lo2f(skw[0]), s1 = hi2f(skw[0]), s2 = lo2f(skw[1]), s3 = hi2f(skw[1]);
        float kv[8];
#pragma unroll
        for (int p = 0; p < 8; ++p) {
            float acc = rb(kk[p + 3] * s3);
            acc = rb(acc + rb(kk[p + 2] * s2));
            acc = rb(acc + rb(kk[p + 1] * s1));
            acc = rb(acc + rb(kk[p] * s0));
            kv[p] = acc * ((p & 1) ? hi2f(tv[p >> 1]) : lo2f(tv[p >> 1]));
        }
        const u32x4 okv = {pk(kv[0], kv[1]), pk(kv[2], kv[3]), pk(kv[4], kv[5]), pk(kv[6], kv[7])};
        const u32 go = ((64 * grp + c) * S + 1984 - 64 * ti + 8 * oct) * 2;
        constexpr int AUX = (PHASE == 0) ? 16 : 0;
        __builtin_amdgcn_raw_buffer_store_b128(okv, sig_rsrc, go, 0, AUX);
        __builtin_amdgcn_raw_buffer_store_b128(tq, sig_rsrc, go + D * S * 2, 0, AUX);
        if (tid == 0) __builtin_amdgcn_raw_buffer_store_b128(lds_ld<u32x4>(L_MISC), g_rsrc, 16 * w, 0, AUX);
    }
    stamp(ws, 2);
    if (PHASE == 1) return;

    {
        const int ch = wv >> 2, kq = wv & 3, r = lane & 15, q = lane >> 4;
        float lh0 = (float)(r < 8 ? r : 120 + r);
        float lh1 = (float)(r < 8 ? 256 + 128 * (r >> 2) + 2 * (r & 3) : 512 + 128 * ((r - 8) >> 1) + 4 * (r & 1));
        float lh2 = (float)(r <= 8 ? 1024 + 128 * r : 0);
        const float lq8 = (float)(8 * r);
        f32x4 acc[3] = {{0, 0, 0, 0}, {0, 0, 0, 0}, {0, 0, 0, 0}};
#pragma unroll 1
        for (int t = 0; t < 2; ++t) {
            u32x4 A0, A1, A2, B;
#pragma unroll
            for (int i = 0; i < 4; ++i) {
                const f32x4 pr = lds_ld<f32x4>(L_KP + (ch * 128 + 16 * (2 * kq + t) + 4 * q + i) * 16);
                {
                    const float e = __builtin_amdgcn_exp2f(pr[0] * lq8), ph = __builtin_amdgcn_fractf(pr[1] * lq8);
                    const float wr = e * __builtin_amdgcn_cosf(ph), wi = e * __builtin_amdgcn_sinf(ph);
                    B[i] = pk(pr[2] * wr - pr[3] * wi, -(pr[2] * wi + pr[3] * wr));
                }
                { const float e = __builtin_amdgcn_exp2f(pr[0] * lh0), ph = __builtin_amdgcn_fractf(pr[1] * lh0); A0[i] = pk(e * __builtin_amdgcn_cosf(ph), e * __builtin_amdgcn_sinf(ph)); }
                { const float e = __builtin_amdgcn_exp2f(pr[0] * lh1), ph = __builtin_amdgcn_fractf(pr[1] * lh1); A1[i] = pk(e * __builtin_amdgcn_cosf(ph), e * __builtin_amdgcn_sinf(ph)); }
                { const float e = __builtin_amdgcn_exp2f(pr[0] * lh2), ph = __builtin_amdgcn_fractf(pr[1] * lh2); A2[i] = pk(e * __builtin_amdgcn_cosf(ph), e * __builtin_amdgcn_sinf(ph)); }
            }
            const bf16x8 b = __builtin_bit_cast(bf16x8, B);
            acc[0] = __builtin_amdgcn_mfma_f32_16x16x32_bf16(__builtin_bit_cast(bf16x8, A0), b, acc[0], 0, 0, 0);
            acc[1] = __builtin_amdgcn_mfma_f32_16x16x32_bf16(__builtin_bit_cast(bf16x8, A1), b, acc[1], 0, 0, 0);
            acc[2] = __builtin_amdgcn_mfma_f32_16x16x32_bf16(__builtin_bit_cast(bf16x8, A2), b, acc[2], 0, 0, 0);
            if (PHASE == 0 && t == 0) {
                asm volatile("s_waitcnt vmcnt(0)" ::: "memory");
                u32 old = 0;
                if (lane == 0) old = __hip_atomic_fetch_add((LAS u32*)(size_t)(L_MISC + 16), 1u, __ATOMIC_RELAXED, __HIP_MEMORY_SCOPE_WORKGROUP);
                old = __builtin_amdgcn_readfirstlane(old);
                if (old == 7 && lane == 0) __hip_atomic_store((gu64*)(ws + WS_FLAGS) + grp * 32 + ti, tag, __ATOMIC_RELAXED, __HIP_MEMORY_SCOPE_AGENT);
                stamp(ws, 3);
            }
        }
        const int b = r, pbase = L_PART + wv * PART_ST;
#pragma unroll
        for (int i = 0; i < 4; ++i) {
            const int row = 4 * q + i;
            const int i0 = (row < 8 ? row : 120 + row) + 8 * b;
            const int i1 = row < 8 ? 256 + 64 * (row >> 2) + (row & 3) + 4 * b : 384 + 32 * ((row - 8) >> 1) + (row & 1) + 2 * b;
            const int i2 = row < 8 ? 512 + 16 * row + b : ((row == 8 && b == 0) ? 640 : 648);
            lds_st<float>(pbase + 4 * i0, acc[0][i]);
            lds_st<float>(pbase + 4 * i1, acc[1][i]);
            lds_st<float>(pbase + 4 * i2, acc[2][i]);
        }
    }
    stamp(ws, 4);
    __syncthreads();
    {
        const int ch = tid >> 8;
#pragma unroll
        for (int m = 0; m < 3; ++m) {
            const int idx = (tid & 255) + 256 * m;
            if (idx <= 640) {
                const int a = L_PART + ch * 4 * PART_ST + 4 * idx;
                const float v = (lds_ld<float>(a) + lds_ld<float>(a + PART_ST)) + (lds_ld<float>(a + 2 * PART_ST) + lds_ld<float>(a + 3 * PART_ST));
                lds_st<u16>(L_KB + ch * 1312 + 2 * idx, (u16)(pk(v, v) & 0xffffu));
            }
        }
    }
    __syncthreads();
    {
        const int ch = tid >> 8, j = tid & 255;
        u32 e[12];
#pragma unroll
        for (int m = 0; m < 11; ++m) {
            const int l = 8 * j + m;
            const int bits = (int)(pk(0.f, (float)l) >> 16);
            int idx = l < 128 ? l : bits - 0x4300 + 128;
            idx = idx > 640 ? 640 : idx;
            e[m] = lds_ld<u16>(L_KB + ch * 1312 + 2 * idx);
        }
        e[11] = 0;
        write_copies(L_KT + (2 * ch + 1) * KT_CC, j, e[0] | (e[1] << 16), e[2] | (e[3] << 16), e[4] | (e[5] << 16), e[6] | (e[7] << 16), e[8] | (e[9] << 16), e[10]);
    }
    stamp(ws, 5);

    if (PHASE == 0) wait_flags((const gu64*)(ws + WS_FLAGS) + grp * 32 + (lane & 31), tag, true);
    stamp(ws, 6);
    {
        constexpr int AUX = (PHASE == 0) ? 16 : 0;
        const int sg = wv >> 1, half = wv & 1;
        const u32 go = (((sg & 1) * D + d0 + (sg >> 1)) * S) * 2 + half * 2048 + lane * 16;
        const u32x4 v0 = __builtin_amdgcn_raw_buffer_load_b128(sig_rsrc, go, 0, AUX);
        const u32x4 v1 = __builtin_amdgcn_raw_buffer_load_b128(sig_rsrc, go + 1024, 0, AUX);
        const int la = L_SIG + sg * SIG_ST + half * 2048 + lane * 16;
        lds_st<u32x4>(la, v0);
        lds_st<u32x4>(la + 1024, v1);
    }
    stamp(ws, 7);
    __syncthreads();
    stamp(ws, 8);

    if (wv < 4) {
        const int r = lane & 15, q = lane >> 4;
        const int a = (r + 1) & 3;
        int ga = L_KT + wv * KT_CC + a * KT_COPY + 2 * (1 + r + 8 * q - a);
        int fa = L_SIG + wv * SIG_ST + 2 * (240 - 16 * r + 8 * q);
        f32x4 acc[8];
#pragma unroll
        for (int i = 0; i < 8; ++i) acc[i] = (f32x4){0, 0, 0, 0};
#pragma unroll 1
        for (int p = 0; p < 8; ++p) {
            u32x4 G[8], F[8];
#pragma unroll
            for (int i = 0; i < 8; ++i) {
                const u32x2 x = lds_ld<u32x2>(ga + 512 * i), y = lds_ld<u32x2>(ga + 512 * i + 8);
                G[i] = (u32x4){x[0], x[1], y[0], y[1]};
                F[i] = lds_ld<u32x4>(fa + 512 * (7 - i));
            }
#pragma unroll
            for (int i = 0; i < 8; ++i)
#pragma unroll
                for (int j = 0; j + i < 8; ++j)
                    acc[i + j] = __builtin_amdgcn_mfma_f32_16x16x32_bf16(__builtin_bit_cast(bf16x8, G[i]), __builtin_bit_cast(bf16x8, F[j]), acc[i + j], 0, 0, 0);
            ga += 64; fa += 64;
        }
        stamp(ws, 9);
#pragma unroll
        for (int ct = 0; ct < 8; ++ct) {
            const u32x2 y = {pk(acc[ct][0], acc[ct][1]), pk(acc[ct][2], acc[ct][3])};
            lds_st<u32x2>(L_T + wv * 4096 + 2 * (256 * ct + 16 * r + 4 * q), y);
        }
    } else {
        if (PHASE == 0) {
            const gu64* f = (const gu64*)(ws + WS_FLAGS) + lane;
            wait_flags(f, tag, true); wait_flags(f + 64, tag, true); wait_flags(f + 128, tag, true); wait_flags(f + 192, tag, true);
            if (w == 0 && tid == 256) __hip_atomic_store((gu64*)(ws + WS_EPOCH), tag, __ATOMIC_RELAXED, __HIP_MEMORY_SCOPE_AGENT);
        }
        constexpr int AUX = (PHASE == 0) ? 16 : 0;
        const u32x4 gv = __builtin_amdgcn_raw_buffer_load_b128(g_rsrc, (tid - 256) * 16, 0, AUX);
        lds_st<u32x4>(L_G + (tid - 256) * 16, gv);
        stamp(ws, 9);
    }
    __syncthreads();
    stamp(ws, 10);

    {
        const int s0 = 4 * tid;
        const u32x2 gg = lds_ld<u32x2>(L_G + 2 * s0);
        float res[2][4];
#pragma unroll
        for (int ch = 0; ch < 2; ++ch) {
            const float sD = bf2f(P.sD[d0 + ch]), dsk = bf2f(P.dsk[d0 + ch]);
            const u32x2 ya = lds_ld<u32x2>(L_T + (2 * ch) * 4096 + 2 * s0), yb = lds_ld<u32x2>(L_T + (2 * ch + 1) * 4096 + 2 * s0);
            const u32x2 kv = lds_ld<u32x2>(L_SIG + (2 * ch) * SIG_ST + 2 * (2044 - s0)), x = lds_ld<u32x2>(L_SIG + (2 * ch + 1) * SIG_ST + 2 * (2044 - s0));
#pragma unroll
            for (int i = 0; i < 4; ++i) {
                const float fya = (i & 1) ? hi2f(ya[i >> 1]) : lo2f(ya[i >> 1]), fyb = (i & 1) ? hi2f(yb[i >> 1]) : lo2f(yb[i >> 1]);
                const int ri = 3 - i;
                const float fkv = (ri & 1) ? hi2f(kv[ri >> 1]) : lo2f(kv[ri >> 1]), fx = (ri & 1) ? hi2f(x[ri >> 1]) : lo2f(x[ri >> 1]);
                const float g = (i & 1) ? hi2f(gg[i >> 1]) : lo2f(gg[i >> 1]);
                const float av = rb(fx * rb(fya + rb(fkv * sD)));
                const float bv = rb(fyb + rb(fx * dsk));
                res[ch][i] = rb(g * av) + rb(rb(1.f - g) * bv);
            }
        }
#pragma unroll
        for (int i = 0; i < 4; ++i) *(u32*)(P.out + (size_t)(s0 + i) * D + d0) = pk(res[0][i], res[1][i]);
    }
    stamp(ws, 11);
}

extern "C" void kernel_launch(void* const* d_in, const int* in_sizes, int n_in, void* d_out, int out_size, void* d_ws, size_t ws_size, hipStream_t stream) {
    Params p;
    p.q = (const u16*)d_in[0]; p.k = (const u16*)d_in[1]; p.v = (const u16*)d_in[2]; p.sk = (const u16*)d_in[3];
    p.ssm = (const u16*)d_in[4]; p.sD = (const u16*)d_in[5];
    p.are = (const u16*)d_in[7]; p.aim = (const u16*)d_in[8]; p.bre = (const u16*)d_in[9]; p.bim = (const u16*)d_in[10];
    p.cre = (const u16*)d_in[11]; p.cim = (const u16*)d_in[12]; p.ldt = (const u16*)d_in[13]; p.dsk = (const u16*)d_in[14]; p.gw = (const u16*)d_in[15];
    p.out = (u16*)d_out; p.ws = (char*)d_ws;
    hipFuncSetAttribute((const void*)fused_kernel<0>, hipFuncAttributeMaxDynamicSharedMemorySize, L_END);
    fused_kernel<0><<<NWG, NT, L_END, stream>>>(p);
}
```

```cpp
#include <hip/hip_runtime.h>
#include <stdint.h>

typedef unsigned short u16;
typedef unsigned int u32;
typedef unsigned long long u64;
typedef u32 u32x4 __attribute__((ext_vector_type(4)));
typedef u32 u32x2 __attribute__((ext_vector_type(2)));
typedef float f32x4 __attribute__((ext_vector_type(4)));
typedef float f32x2 __attribute__((ext_vector_type(2)));
typedef __bf16 bf16x2 __attribute__((ext_vector_type(2)));
typedef __bf16 bf16x8 __attribute__((ext_vector_type(8)));
#define GAS __attribute__((address_space(1)))
#define LAS __attribute__((address_space(3)))
typedef GAS u64 gu64;

#define STAMPS 0

constexpr int S = 2048, D = 512, NS = 128;
constexpr int NWG = 256, NT = 512;

constexpr size_t WS_EPOCH = 0;
constexpr size_t WS_FLAG1 = 4096;
constexpr size_t WS_G     = 16384;
constexpr size_t WS_SIG   = 65536;
constexpr size_t WS_STAMP = 8u << 20;

constexpr int KT_COPY = 4160;
constexpr int KT_CC   = 4 * KT_COPY;
constexpr int TB_ROW  = 528, TB_CH = 64 * TB_ROW;
constexpr int L_X     = 0;
constexpr int L_Y     = L_X + 2 * KT_CC;
constexpr int L_KTA   = L_X + 2 * TB_CH;
constexpr int SIG_ST  = 4608;
constexpr int L_SIG   = L_KTA + 2 * KT_CC;
constexpr int L_T     = L_SIG + 4 * SIG_ST;
constexpr int L_H     = L_T + 3 * 8192;
constexpr int L_KB    = L_H + 1024;
constexpr int L_MISC  = L_KB + 2 * 1312;
constexpr int L_END   = L_MISC + 64;
static_assert(L_Y + 16384 <= L_KTA, "Y overlay");

__device__ __forceinline__ u32 pk(float lo, float hi) { f32x2 v = {lo, hi}; bf16x2 b = __builtin_convertvector(v, bf16x2); return __builtin_bit_cast(u32, b); }
__device__ __forceinline__ float rb(float x) { return __uint_as_float(pk(0.f, x)); }
__device__ __forceinline__ float lo2f(u32 d) { return __uint_as_float(d << 16); }
__device__ __forceinline__ float hi2f(u32 d) { return __uint_as_float(d & 0xffff0000u); }
__device__ __forceinline__ float bf2f(u16 v) { return __uint_as_float(((u32)v) << 16); }

template <typename T> __device__ __forceinline__ T lds_ld(int off) { return *(const LAS T*)(size_t)(u32)off; }
template <typename T> __device__ __forceinline__ void lds_st(int off, T v) { *(LAS T*)(size_t)(u32)off = v; }

struct Params {
    const u16 *q, *k, *v, *sk, *ssm, *sD, *are, *aim, *bre, *bim, *cre, *cim, *ldt, *dsk, *gw;
    u16* out; char* ws;
};

__device__ __forceinline__ void stamp(char* ws, int slot) {
}

__device__ __forceinline__ u32x4 tr8(u32x4 d, u32 sel) {
    u32 e[4];
#pragma unroll
    for (int m = 0; m < 4; ++m) {
        u32 p = __builtin_amdgcn_update_dpp(0u, d[m], 0x128, 0xf, 0xf, true);
        e[m] = __builtin_amdgcn_perm(p, d[m], sel);
    }
    auto a = __builtin_amdgcn_permlane16_swap(e[0], e[1], false, false);
    auto b = __builtin_amdgcn_permlane16_swap(e[2], e[3], false, false);
    auto c = __builtin_amdgcn_permlane32_swap(a[0], b[0], false, false);
    auto f = __builtin_amdgcn_permlane32_swap(a[1], b[1], false, false);
    u32x4 o = {f[1], c[1], f[0], c[0]};
    return o;
}

__device__ __forceinline__ void write_copies(int base, int j, u32 d0, u32 d1, u32 d2, u32 d3, u32 d4, u32 d5) {
    u32 a10 = __builtin_amdgcn_alignbit(d1, d0, 16), a21 = __builtin_amdgcn_alignbit(d2, d1, 16), a32 = __builtin_amdgcn_alignbit(d3, d2, 16),
        a43 = __builtin_amdgcn_alignbit(d4, d3, 16), a54 = __builtin_amdgcn_alignbit(d5, d4, 16);
    int o = base + 32 + 16 * j;
    lds_st<u32x4>(o, (u32x4){d0, d1, d2, d3});
    lds_st<u32x4>(o + KT_COPY, (u32x4){a10, a21, a32, a43});
    lds_st<u32x4>(o + 2 * KT_COPY, (u32x4){d1, d2, d3, d4});
    lds_st<u32x4>(o + 3 * KT_COPY, (u32x4){a21, a32, a43, a54});
    if (j == 0) {
        const u32x4 z = {0, 0, 0, 0};
#pragma unroll
        for (int a = 0; a < 4; ++a) lds_st<u32x4>(base + a * KT_COPY, z);
        lds_st<u32x4>(base + 16, z);
        lds_st<u32x4>(base + 16 + KT_COPY, (u32x4){0, 0, 0, d0 << 16});
        lds_st<u32x4>(base + 16 + 2 * KT_COPY, (u32x4){0, 0, 0, d0});
        lds_st<u32x4>(base + 16 + 3 * KT_COPY, (u32x4){0, 0, d0 << 16, a10});
    }
}

__device__ __forceinline__ bool wait_flags(const gu64* f, u64 tag) {
    for (u32 spins = 0; spins < (1u << 18); ++spins) {
        u64 x = __hip_atomic_load(f, __ATOMIC_RELAXED, __HIP_MEMORY_SCOPE_AGENT);
        if (__all(x == tag)) return true;
        __builtin_amdgcn_s_sleep(1);
    }
    return false;
}

typedef f32x2 cpx;
__device__ __forceinline__ cpx cmul(cpx a, cpx b) {
    cpx r;
    asm("v_pk_mul_f32 %0, %1, %2 op_sel_hi:[0,1]\n\tv_pk_fma_f32 %0, %1, %2, %0 op_sel:[1,1,0] op_sel_hi:[1,0,1] neg_lo:[1,0,0]" : "=&v"(r) : "v"(a), "v"(b));
    return r;
}
__device__ __forceinline__ cpx cexp_rev(float lm, float rev) {
    const float e = __builtin_amdgcn_exp2f(lm);
    return (cpx){e * __builtin_amdgcn_cosf(rev), e * __builtin_amdgcn_sinf(rev)};
}
__device__ __forceinline__ u32 pkc(cpx a) { return pk(a.x, a.y); }
__device__ __forceinline__ float ffma(float a, float b, float c) { float r; asm("v_fma_f32 %0, %1, %2, %3" : "=v"(r) : "v"(a), "v"(b), "v"(c)); return r; }
__device__ __forceinline__ float fmul(float a, float b) { float r; asm("v_mul_f32 %0, %1, %2" : "=v"(r) : "v"(a), "v"(b)); return r; }
template <int O0, int O1, int O2, int O3>
__device__ __forceinline__ void st4_addtid(u32 base, u32 a, u32 b, u32 c, u32 d) {
    u32 keep;
    asm volatile("s_mov_b32 %0, m0\n\ts_mov_b32 m0, %1\n\ts_nop 0\n\t"
                 "ds_write_addtid_b32 %2 offset:%6\n\tds_write_addtid_b32 %3 offset:%7\n\tds_write_addtid_b32 %4 offset:%8\n\tds_write_addtid_b32 %5 offset:%9\n\t"
                 "s_mov_b32 m0, %0"
                 : "=&s"(keep) : "s"(base), "v"(a), "v"(b), "v"(c), "v"(d), "i"(O0), "i"(O1), "i"(O2), "i"(O3) : "memory");
}

__device__ __forceinline__ void conv_all(int ga, int gb, int fa, int ya, f32x4& a4, f32x4& a5, f32x4& a6, f32x4& a7) {
    u32 cnt;
    asm volatile(
        "s_setprio 3\n"
        "ds_read_b64 v[128:129], %[ga] offset:0\n"
        "ds_read_b64 v[130:131], %[gb] offset:0\n"
        "ds_read_b128 v[144:147], %[fa] offset:3584\n"
        "ds_read_b128 v[148:151], %[fa] offset:3072\n"
        "ds_read_b128 v[152:155], %[fa] offset:2560\n"
        "ds_read_b128 v[156:159], %[fa] offset:2048\n"
        "ds_read_b64 v[132:133], %[ga] offset:512\n"
        "ds_read_b64 v[134:135], %[gb] offset:512\n"
        "ds_read_b64 v[136:137], %[ga] offset:1024\n"
        "ds_read_b64 v[138:139], %[gb] offset:1024\n"
        "ds_read_b64 v[140:141], %[ga] offset:1536\n"
        "ds_read_b64 v[142:143], %[gb] offset:1536\n"
        "ds_read_b64 v[160:161], %[ga] offset:64\n"
        "ds_read_b64 v[162:163], %[gb] offset:64\n"
        "ds_read_b128 v[176:179], %[fa] offset:3648\n"
        "ds_read_b128 v[180:183], %[fa] offset:3136\n"
        "ds_read_b128 v[184:187], %[fa] offset:2624\n"
        "ds_read_b128 v[188:191], %[fa] offset:2112\n"
        "ds_read_b64 v[164:165], %[ga] offset:576\n"
        "ds_read_b64 v[166:167], %[gb] offset:576\n"
        "ds_read_b64 v[168:169], %[ga] offset:1088\n"
        "ds_read_b64 v[170:171], %[gb] offset:1088\n"
        "ds_read_b64 v[172:173], %[ga] offset:1600\n"
        "ds_read_b64 v[174:175], %[gb] offset:1600\n"
        "s_waitcnt lgkmcnt(15)\n"
        "v_mfma_f32_16x16x32_bf16 v[112:115], v[128:131], v[144:147], 0\n"
        "ds_read_b64 v[192:193], %[ga] offset:128\n"
        "ds_read_b64 v[194:195], %[gb] offset:128\n"
        "v_mfma_f32_16x16x32_bf16 v[116:119], v[128:131], v[148:151], 0\n"
        "ds_read_b128 v[208:211], %[fa] offset:3712\n"
        "v_mfma_f32_16x16x32_bf16 v[120:123], v[128:131], v[152:155], 0\n"
        "ds_read_b128 v[212:215], %[fa] offset:3200\n"
        "v_mfma_f32_16x16x32_bf16 v[124:127], v[128:131], v[156:159], 0\n"
        "ds_read_b128 v[216:219], %[fa] offset:2688\n"
        "ds_read_b128 v[220:223], %[fa] offset:2176\n"
        "v_mfma_f32_16x16x32_bf16 v[116:119], v[132:135], v[144:147], v[116:119]\n"
        "ds_read_b64 v[196:197], %[ga] offset:640\n"
        "v_mfma_f32_16x16x32_bf16 v[120:123], v[132:135], v[148:151], v[120:123]\n"
        "ds_read_b64 v[198:199], %[gb] offset:640\n"
        "v_mfma_f32_16x16x32_bf16 v[124:127], v[132:135], v[152:155], v[124:127]\n"
        "ds_read_b64 v[200:201], %[ga] offset:1152\n"
        "ds_read_b64 v[202:203], %[gb] offset:1152\n"
        "s_waitcnt lgkmcnt(15)\n"
        "v_mfma_f32_16x16x32_bf16 v[120:123], v[136:139], v[144:147], v[120:123]\n"
        "ds_read_b64 v[204:205], %[ga] offset:1664\n"
        "v_mfma_f32_16x16x32_bf16 v[124:127], v[136:139], v[148:151], v[124:127]\n"
        "ds_read_b64 v[206:207], %[gb] offset:1664\n"
        "v_mfma_f32_16x16x32_bf16 v[124:127], v[140:143], v[144:147], v[124:127]\n"
        "ds_read_b64 v[224:225], %[ga] offset:192\n"
        "ds_read_b64 v[226:227], %[gb] offset:192\n"
        "v_mfma_f32_16x16x32_bf16 v[112:115], v[160:163], v[176:179], v[112:115]\n"
        "ds_read_b128 v[240:243], %[fa] offset:3776\n"
        "v_mfma_f32_16x16x32_bf16 v[116:119], v[160:163], v[180:183], v[116:119]\n"
        "ds_read_b128 v[244:247], %[fa] offset:3264\n"
        "v_mfma_f32_16x16x32_bf16 v[120:123], v[160:163], v[184:187], v[120:123]\n"
        "ds_read_b128 v[248:251], %[fa] offset:2752\n"
        "ds_read_b128 v[252:255], %[fa] offset:2240\n"
        "v_mfma_f32_16x16x32_bf16 v[124:127], v[160:163], v[188:191], v[124:127]\n"
        "ds_read_b64 v[228:229], %[ga] offset:704\n"
        "s_waitcnt lgkmcnt(15)\n"
        "v_mfma_f32_16x16x32_bf16 v[116:119], v[164:167], v[176:179], v[116:119]\n"
        "ds_read_b64 v[230:231], %[gb] offset:704\n"
        "v_mfma_f32_16x16x32_bf16 v[120:123], v[164:167], v[180:183], v[120:123]\n"
        "ds_read_b64 v[232:233], %[ga] offset:1216\n"
        "ds_read_b64 v[234:235], %[gb] offset:1216\n"
        "v_mfma_f32_16x16x32_bf16 v[124:127], v[164:167], v[184:187], v[124:127]\n"
        "ds_read_b64 v[236:237], %[ga] offset:1728\n"
        "v_mfma_f32_16x16x32_bf16 v[120:123], v[168:171], v[176:179], v[120:123]\n"
        "ds_read_b64 v[238:239], %[gb] offset:1728\n"
        "v_mfma_f32_16x16x32_bf16 v[124:127], v[168:171], v[180:183], v[124:127]\n"
        "v_mfma_f32_16x16x32_bf16 v[124:127], v[172:175], v[176:179], v[124:127]\n"
        "v_mfma_f32_16x16x32_bf16 v[112:115], v[192:195], v[208:211], v[112:115]\n"
        "ds_read_b64 v[128:129], %[ga] offset:256\n"
        "ds_read_b64 v[130:131], %[gb] offset:256\n"
        "v_mfma_f32_16x16x32_bf16 v[116:119], v[192:195], v[212:215], v[116:119]\n"
        "ds_read_b128 v[144:147], %[fa] offset:3840\n"
        "s_waitcnt lgkmcnt(15)\n"
        "v_mfma_f32_16x16x32_bf16 v[120:123], v[192:195], v[216:219], v[120:123]\n"
        "ds_read_b128 v[148:151], %[fa] offset:3328\n"
        "v_mfma_f32_16x16x32_bf16 v[124:127], v[192:195], v[220:223], v[124:127]\n"
        "ds_read_b128 v[152:155], %[fa] offset:2816\n"
        "ds_read_b128 v[156:159], %[fa] offset:2304\n"
        "v_mfma_f32_16x16x32_bf16 v[116:119], v[196:199], v[208:211], v[116:119]\n"
        "ds_read_b64 v[132:133], %[ga] offset:768\n"
        "v_mfma_f32_16x16x32_bf16 v[120:123], v[196:199], v[212:215], v[120:123]\n"
        "ds_read_b64 v[134:135], %[gb] offset:768\n"
        "v_mfma_f32_16x16x32_bf16 v[124:127], v[196:199], v[216:219], v[124:127]\n"
        "ds_read_b64 v[136:137], %[ga] offset:1280\n"
        "ds_read_b64 v[138:139], %[gb] offset:1280\n"
        "v_mfma_f32_16x16x32_bf16 v[120:123], v[200:203], v[208:211], v[120:123]\n"
        "ds_read_b64 v[140:141], %[ga] offset:1792\n"
        "v_mfma_f32_16x16x32_bf16 v[124:127], v[200:203], v[212:215], v[124:127]\n"
        "ds_read_b64 v[142:143], %[gb] offset:1792\n"
        "v_mfma_f32_16x16x32_bf16 v[124:127], v[204:207], v[208:211], v[124:127]\n"
        "ds_read_b64 v[160:161], %[ga] offset:320\n"
        "ds_read_b64 v[162:163], %[gb] offset:320\n"
        "s_waitcnt lgkmcnt(15)\n"
        "v_mfma_f32_16x16x32_bf16 v[112:115], v[224:227], v[240:243], v[112:115]\n"
        "ds_read_b128 v[176:179], %[fa] offset:3904\n"
        "v_mfma_f32_16x16x32_bf16 v[116:119], v[224:227], v[244:247], v[116:119]\n"
        "ds_read_b128 v[180:183], %[fa] offset:3392\n"
        "v_mfma_f32_16x16x32_bf16 v[120:123], v[224:227], v[248:251], v[120:123]\n"
        "ds_read_b128 v[184:187], %[fa] offset:2880\n"
        "ds_read_b128 v[188:191], %[fa] offset:2368\n"
        "v_mfma_f32_16x16x32_bf16 v[124:127], v[224:227], v[252:255], v[124:127]\n"
        "ds_read_b64 v[164:165], %[ga] offset:832\n"
        "v_mfma_f32_16x16x32_bf16 v[116:119], v[228:231], v[240:243], v[116:119]\n"
        "ds_read_b64 v[166:167], %[gb] offset:832\n"
        "v_mfma_f32_16x16x32_bf16 v[120:123], v[228:231], v[244:247], v[120:123]\n"
        "ds_read_b64 v[168:169], %[ga] offset:1344\n"
        "ds_read_b64 v[170:171], %[gb] offset:1344\n"
        "v_mfma_f32_16x16x32_bf16 v[124:127], v[228:231], v[248:251], v[124:127]\n"
        "ds_read_b64 v[172:173], %[ga] offset:1856\n"
        "v_mfma_f32_16x16x32_bf16 v[120:123], v[232:235], v[240:243], v[120:123]\n"
        "ds_read_b64 v[174:175], %[gb] offset:1856\n"
        "v_mfma_f32_16x16x32_bf16 v[124:127], v[232:235], v[244:247], v[124:127]\n"
        "s_waitcnt lgkmcnt(15)\n"
        "v_mfma_f32_16x16x32_bf16 v[124:127], v[236:239], v[240:243], v[124:127]\n"
        "v_mfma_f32_16x16x32_bf16 v[112:115], v[128:131], v[144:147], v[112:115]\n"
        "ds_read_b64 v[192:193], %[ga] offset:384\n"
        "ds_read_b64 v[194:195], %[gb] offset:384\n"
        "v_mfma_f32_16x16x32_bf16 v[116:119], v[128:131], v[148:151], v[116:119]\n"
        "ds_read_b128 v[208:211], %[fa] offset:3968\n"
        "v_mfma_f32_16x16x32_bf16 v[120:123], v[128:131], v[152:155], v[120:123]\n"
        "ds_read_b128 v[212:215], %[fa] offset:3456\n"
        "v_mfma_f32_16x16x32_bf16 v[124:127], v[128:131], v[156:159], v[124:127]\n"
        "ds_read_b128 v[216:219], %[fa] offset:2944\n"
        "ds_read_b128 v[220:223], %[fa] offset:2432\n"
        "v_mfma_f32_16x16x32_bf16 v[116:119], v[132:135], v[144:147], v[116:119]\n"
        "ds_read_b64 v[196:197], %[ga] offset:896\n"
        "v_mfma_f32_16x16x32_bf16 v[120:123], v[132:135], v[148:151], v[120:123]\n"
        "ds_read_b64 v[198:199], %[gb] offset:896\n"
        "v_mfma_f32_16x16x32_bf16 v[124:127], v[132:135], v[152:155], v[124:127]\n"
        "ds_read_b64 v[200:201], %[ga] offset:1408\n"
        "ds_read_b64 v[202:203], %[gb] offset:1408\n"
        "s_waitcnt lgkmcnt(15)\n"
        "v_mfma_f32_16x16x32_bf16 v[120:123], v[136:139], v[144:147], v[120:123]\n"
        "ds_read_b64 v[204:205], %[ga] offset:1920\n"
        "v_mfma_f32_16x16x32_bf16 v[124:127], v[136:139], v[148:151], v[124:127]\n"
        "ds_read_b64 v[206:207], %[gb] offset:1920\n"
        "v_mfma_f32_16x16x32_bf16 v[124:127], v[140:143], v[144:147], v[124:127]\n"
        "ds_read_b64 v[224:225], %[ga] offset:448\n"
        "ds_read_b64 v[226:227], %[gb] offset:448\n"
        "v_mfma_f32_16x16x32_bf16 v[112:115], v[160:163], v[176:179], v[112:115]\n"
        "ds_read_b128 v[240:243], %[fa] offset:4032\n"
        "v_mfma_f32_16x16x32_bf16 v[116:119], v[160:163], v[180:183], v[116:119]\n"
        "ds_read_b128 v[244:247], %[fa] offset:3520\n"
        "v_mfma_f32_16x16x32_bf16 v[120:123], v[160:163], v[184:187], v[120:123]\n"
        "ds_read_b128 v[248:251], %[fa] offset:3008\n"
        "ds_read_b128 v[252:255], %[fa] offset:2496\n"
        "v_mfma_f32_16x16x32_bf16 v[124:127], v[160:163], v[188:191], v[124:127]\n"
        "ds_read_b64 v[228:229], %[ga] offset:960\n"
        "s_waitcnt lgkmcnt(15)\n"
        "v_mfma_f32_16x16x32_bf16 v[116:119], v[164:167], v[176:179], v[116:119]\n"
        "ds_read_b64 v[230:231], %[gb] offset:960\n"
        "v_mfma_f32_16x16x32_bf16 v[120:123], v[164:167], v[180:183], v[120:123]\n"
        "ds_read_b64 v[232:233], %[ga] offset:1472\n"
        "ds_read_b64 v[234:235], %[gb] offset:1472\n"
        "v_mfma_f32_16x16x32_bf16 v[124:127], v[164:167], v[184:187], v[124:127]\n"
        "ds_read_b64 v[236:237], %[ga] offset:1984\n"
        "v_mfma_f32_16x16x32_bf16 v[120:123], v[168:171], v[176:179], v[120:123]\n"
        "ds_read_b64 v[238:239], %[gb] offset:1984\n"
        "v_mfma_f32_16x16x32_bf16 v[124:127], v[168:171], v[180:183], v[124:127]\n"
        "v_mfma_f32_16x16x32_bf16 v[124:127], v[172:175], v[176:179], v[124:127]\n"
        "v_mfma_f32_16x16x32_bf16 v[112:115], v[192:195], v[208:211], v[112:115]\n"
        "ds_read_b64 v[128:129], %[ga] offset:0\n"
        "ds_read_b64 v[130:131], %[gb] offset:0\n"
        "v_mfma_f32_16x16x32_bf16 v[116:119], v[192:195], v[212:215], v[116:119]\n"
        "ds_read_b128 v[176:179], %[fa] offset:1536\n"
        "s_waitcnt lgkmcnt(15)\n"
        "v_mfma_f32_16x16x32_bf16 v[120:123], v[192:195], v[216:219], v[120:123]\n"
        "ds_read_b128 v[180:183], %[fa] offset:1024\n"
        "v_mfma_f32_16x16x32_bf16 v[124:127], v[192:195], v[220:223], v[124:127]\n"
        "ds_read_b128 v[184:187], %[fa] offset:512\n"
        "ds_read_b128 v[188:191], %[fa] offset:0\n"
        "v_mfma_f32_16x16x32_bf16 v[116:119], v[196:199], v[208:211], v[116:119]\n"
        "ds_read_b64 v[132:133], %[ga] offset:512\n"
        "v_mfma_f32_16x16x32_bf16 v[120:123], v[196:199], v[212:215], v[120:123]\n"
        "ds_read_b64 v[134:135], %[gb] offset:512\n"
        "v_mfma_f32_16x16x32_bf16 v[124:127], v[196:199], v[216:219], v[124:127]\n"
        "ds_read_b128 v[172:175], %[fa] offset:2048\n"
        "ds_read_b64 v[136:137], %[ga] offset:1024\n"
        "v_mfma_f32_16x16x32_bf16 v[120:123], v[200:203], v[208:211], v[120:123]\n"
        "ds_read_b64 v[138:139], %[gb] offset:1024\n"
        "v_mfma_f32_16x16x32_bf16 v[124:127], v[200:203], v[212:215], v[124:127]\n"
        "ds_read_b128 v[168:171], %[fa] offset:2560\n"
        "v_mfma_f32_16x16x32_bf16 v[124:127], v[204:207], v[208:211], v[124:127]\n"
        "ds_read_b64 v[140:141], %[ga] offset:1536\n"
        "ds_read_b64 v[142:143], %[gb] offset:1536\n"
        "s_waitcnt lgkmcnt(15)\n"
        "v_mfma_f32_16x16x32_bf16 v[112:115], v[224:227], v[240:243], v[112:115]\n"
        "ds_read_b128 v[164:167], %[fa] offset:3072\n"
        "v_mfma_f32_16x16x32_bf16 v[116:119], v[224:227], v[244:247], v[116:119]\n"
        "ds_read_b64 v[144:145], %[ga] offset:2048\n"
        "v_mfma_f32_16x16x32_bf16 v[120:123], v[224:227], v[248:251], v[120:123]\n"
        "ds_read_b64 v[146:147], %[gb] offset:2048\n"
        "ds_read_b128 v[160:163], %[fa] offset:3584\n"
        "v_mfma_f32_16x16x32_bf16 v[124:127], v[224:227], v[252:255], v[124:127]\n"
        "ds_read_b64 v[148:149], %[ga] offset:2560\n"
        "v_mfma_f32_16x16x32_bf16 v[116:119], v[228:231], v[240:243], v[116:119]\n"
        "ds_read_b64 v[150:151], %[gb] offset:2560\n"
        "v_mfma_f32_16x16x32_bf16 v[120:123], v[228:231], v[244:247], v[120:123]\n"
        "ds_read_b64 v[152:153], %[ga] offset:3072\n"
        "ds_read_b64 v[154:155], %[gb] offset:3072\n"
        "v_mfma_f32_16x16x32_bf16 v[124:127], v[228:231], v[248:251], v[124:127]\n"
        "ds_read_b64 v[156:157], %[ga] offset:3584\n"
        "v_mfma_f32_16x16x32_bf16 v[120:123], v[232:235], v[240:243], v[120:123]\n"
        "ds_read_b64 v[158:159], %[gb] offset:3584\n"
        "v_mfma_f32_16x16x32_bf16 v[124:127], v[232:235], v[244:247], v[124:127]\n"
        "s_waitcnt lgkmcnt(15)\n"
        "v_mfma_f32_16x16x32_bf16 v[124:127], v[236:239], v[240:243], v[124:127]\n"
        "v_mfma_f32_16x16x32_bf16 %[a4], v[128:131], v[176:179], 0\n"
        "ds_read_b64 v[192:193], %[ga] offset:64\n"
        "v_mfma_f32_16x16x32_bf16 %[a5], v[128:131], v[180:183], 0\n"
        "ds_read_b64 v[194:195], %[gb] offset:64\n"
        "v_mfma_f32_16x16x32_bf16 %[a6], v[128:131], v[184:187], 0\n"
        "ds_read_b128 v[240:243], %[fa] offset:1600\n"
        "v_mfma_f32_16x16x32_bf16 %[a7], v[128:131], v[188:191], 0\n"
        "ds_read_b128 v[244:247], %[fa] offset:1088\n"
        "v_mfma_f32_16x16x32_bf16 %[a7], v[132:135], v[184:187], %[a7]\n"
        "ds_read_b128 v[248:251], %[fa] offset:576\n"
        "v_mfma_f32_16x16x32_bf16 %[a6], v[132:135], v[180:183], %[a6]\n"
        "ds_read_b128 v[252:255], %[fa] offset:64\n"
        "v_mfma_f32_16x16x32_bf16 %[a5], v[132:135], v[176:179], %[a5]\n"
        "ds_read_b64 v[196:197], %[ga] offset:576\n"
        "v_mfma_f32_16x16x32_bf16 %[a4], v[132:135], v[172:175], %[a4]\n"
        "ds_read_b64 v[198:199], %[gb] offset:576\n"
        "s_waitcnt lgkmcnt(15)\n"
        "v_mfma_f32_16x16x32_bf16 %[a7], v[136:139], v[180:183], %[a7]\n"
        "ds_read_b128 v[236:239], %[fa] offset:2112\n"
        "v_cvt_pk_bf16_f32 v104, v112, v113\n"
        "v_cvt_pk_bf16_f32 v105, v114, v115\n"
        "v_mfma_f32_16x16x32_bf16 %[a6], v[136:139], v[176:179], %[a6]\n"
        "ds_read_b64 v[200:201], %[ga] offset:1088\n"
        "ds_write_b64 %[ya], v[104:105] offset:0\n"
        "v_mfma_f32_16x16x32_bf16 %[a5], v[136:139], v[172:175], %[a5]\n"
        "ds_read_b64 v[202:203], %[gb] offset:1088\n"
        "v_cvt_pk_bf16_f32 v106, v116, v117\n"
        "v_cvt_pk_bf16_f32 v107, v118, v119\n"
        "v_mfma_f32_16x16x32_bf16 %[a4], v[136:139], v[168:171], %[a4]\n"
        "ds_read_b128 v[232:235], %[fa] offset:2624\n"
        "ds_write_b64 %[ya], v[106:107] offset:512\n"
        "v_mfma_f32_16x16x32_bf16 %[a7], v[140:143], v[176:179], %[a7]\n"
        "ds_read_b64 v[204:205], %[ga] offset:1600\n"
        "v_cvt_pk_bf16_f32 v108, v120, v121\n"
        "v_cvt_pk_bf16_f32 v109, v122, v123\n"
        "v_mfma_f32_16x16x32_bf16 %[a6], v[140:143], v[172:175], %[a6]\n"
        "ds_read_b64 v[206:207], %[gb] offset:1600\n"
        "ds_write_b64 %[ya], v[108:109] offset:1024\n"
        "v_mfma_f32_16x16x32_bf16 %[a5], v[140:143], v[168:171], %[a5]\n"
        "ds_read_b128 v[228:231], %[fa] offset:3136\n"
        "v_cvt_pk_bf16_f32 v110, v124, v125\n"
        "v_cvt_pk_bf16_f32 v111, v126, v127\n"
        "v_mfma_f32_16x16x32_bf16 %[a4], v[140:143], v[164:167], %[a4]\n"
        "ds_read_b64 v[208:209], %[ga] offset:2112\n"
        "ds_write_b64 %[ya], v[110:111] offset:1536\n"
        "v_mfma_f32_16x16x32_bf16 %[a7], v[144:147], v[172:175], %[a7]\n"
        "ds_read_b64 v[210:211], %[gb] offset:2112\n"
        "v_mfma_f32_16x16x32_bf16 %[a6], v[144:147], v[168:171], %[a6]\n"
        "ds_read_b128 v[224:227], %[fa] offset:3648\n"
        "v_mfma_f32_16x16x32_bf16 %[a5], v[144:147], v[164:167], %[a5]\n"
        "ds_read_b64 v[212:213], %[ga] offset:2624\n"
        "s_waitcnt lgkmcnt(15)\n"
        "v_mfma_f32_16x16x32_bf16 %[a4], v[144:147], v[160:163], %[a4]\n"
        "ds_read_b64 v[214:215], %[gb] offset:2624\n"
        "v_mfma_f32_16x16x32_bf16 %[a5], v[148:151], v[160:163], %[a5]\n"
        "ds_read_b64 v[216:217], %[ga] offset:3136\n"
        "v_mfma_f32_16x16x32_bf16 %[a6], v[148:151], v[164:167], %[a6]\n"
        "ds_read_b64 v[218:219], %[gb] offset:3136\n"
        "v_mfma_f32_16x16x32_bf16 %[a7], v[148:151], v[168:171], %[a7]\n"
        "ds_read_b64 v[220:221], %[ga] offset:3648\n"
        "v_mfma_f32_16x16x32_bf16 %[a6], v[152:155], v[160:163], %[a6]\n"
        "ds_read_b64 v[222:223], %[gb] offset:3648\n"
        "v_mfma_f32_16x16x32_bf16 %[a7], v[152:155], v[164:167], %[a7]\n"
        "v_mfma_f32_16x16x32_bf16 %[a7], v[156:159], v[160:163], %[a7]\n"
        "s_waitcnt lgkmcnt(4)\n"
        "s_barrier\n"
        "s_mov_b32 %[cnt], 3\n"
        "1:\n"
        "v_mfma_f32_16x16x32_bf16 %[a4], v[192:195], v[240:243], %[a4]\n"
        "ds_read_b64 v[128:129], %[ga] offset:128\n"
        "v_mfma_f32_16x16x32_bf16 %[a5], v[192:195], v[244:247], %[a5]\n"
        "ds_read_b64 v[130:131], %[gb] offset:128\n"
        "v_mfma_f32_16x16x32_bf16 %[a6], v[192:195], v[248:251], %[a6]\n"
        "ds_read_b128 v[176:179], %[fa] offset:1664\n"
        "v_mfma_f32_16x16x32_bf16 %[a7], v[192:195], v[252:255], %[a7]\n"
        "ds_read_b128 v[180:183], %[fa] offset:1152\n"
        "v_mfma_f32_16x16x32_bf16 %[a7], v[196:199], v[248:251], %[a7]\n"
        "ds_read_b128 v[184:187], %[fa] offset:640\n"
        "v_mfma_f32_16x16x32_bf16 %[a6], v[196:199], v[244:247], %[a6]\n"
        "ds_read_b128 v[188:191], %[fa] offset:128\n"
        "v_mfma_f32_16x16x32_bf16 %[a5], v[196:199], v[240:243], %[a5]\n"
        "ds_read_b64 v[132:133], %[ga] offset:640\n"
        "v_mfma_f32_16x16x32_bf16 %[a4], v[196:199], v[236:239], %[a4]\n"
        "ds_read_b64 v[134:135], %[gb] offset:640\n"
        "v_mfma_f32_16x16x32_bf16 %[a7], v[200:203], v[244:247], %[a7]\n"
        "ds_read_b128 v[172:175], %[fa] offset:2176\n"
        "v_mfma_f32_16x16x32_bf16 %[a6], v[200:203], v[240:243], %[a6]\n"
        "ds_read_b64 v[136:137], %[ga] offset:1152\n"
        "v_mfma_f32_16x16x32_bf16 %[a5], v[200:203], v[236:239], %[a5]\n"
        "ds_read_b64 v[138:139], %[gb] offset:1152\n"
        "v_mfma_f32_16x16x32_bf16 %[a4], v[200:203], v[232:235], %[a4]\n"
        "ds_read_b128 v[168:171], %[fa] offset:2688\n"
        "v_mfma_f32_16x16x32_bf16 %[a7], v[204:207], v[240:243], %[a7]\n"
        "ds_read_b64 v[140:141], %[ga] offset:1664\n"
        "v_mfma_f32_16x16x32_bf16 %[a6], v[204:207], v[236:239], %[a6]\n"
        "ds_read_b64 v[142:143], %[gb] offset:1664\n"
        "v_mfma_f32_16x16x32_bf16 %[a5], v[204:207], v[232:235], %[a5]\n"
        "ds_read_b128 v[164:167], %[fa] offset:3200\n"
        "v_mfma_f32_16x16x32_bf16 %[a4], v[204:207], v[228:231], %[a4]\n"
        "ds_read_b64 v[144:145], %[ga] offset:2176\n"
        "s_waitcnt lgkmcnt(15)\n"
        "v_mfma_f32_16x16x32_bf16 %[a7], v[208:211], v[236:239], %[a7]\n"
        "ds_read_b64 v[146:147], %[gb] offset:2176\n"
        "v_mfma_f32_16x16x32_bf16 %[a6], v[208:211], v[232:235], %[a6]\n"
        "ds_read_b128 v[160:163], %[fa] offset:3712\n"
        "v_mfma_f32_16x16x32_bf16 %[a5], v[208:211], v[228:231], %[a5]\n"
        "ds_read_b64 v[148:149], %[ga] offset:2688\n"
        "v_mfma_f32_16x16x32_bf16 %[a4], v[208:211], v[224:227], %[a4]\n"
        "ds_read_b64 v[150:151], %[gb] offset:2688\n"
        "v_mfma_f32_16x16x32_bf16 %[a5], v[212:215], v[224:227], %[a5]\n"
        "ds_read_b64 v[152:153], %[ga] offset:3200\n"
        "v_mfma_f32_16x16x32_bf16 %[a6], v[212:215], v[228:231], %[a6]\n"
        "ds_read_b64 v[154:155], %[gb] offset:3200\n"
        "v_mfma_f32_16x16x32_bf16 %[a7], v[212:215], v[232:235], %[a7]\n"
        "ds_read_b64 v[156:157], %[ga] offset:3712\n"
        "v_mfma_f32_16x16x32_bf16 %[a6], v[216:219], v[224:227], %[a6]\n"
        "ds_read_b64 v[158:159], %[gb] offset:3712\n"
        "v_mfma_f32_16x16x32_bf16 %[a7], v[216:219], v[228:231], %[a7]\n"
        "v_mfma_f32_16x16x32_bf16 %[a7], v[220:223], v[224:227], %[a7]\n"
        "s_waitcnt lgkmcnt(15)\n"
        "v_mfma_f32_16x16x32_bf16 %[a4], v[128:131], v[176:179], %[a4]\n"
        "ds_read_b64 v[192:193], %[ga] offset:192\n"
        "v_mfma_f32_16x16x32_bf16 %[a5], v[128:131], v[180:183], %[a5]\n"
        "ds_read_b64 v[194:195], %[gb] offset:192\n"
        "v_mfma_f32_16x16x32_bf16 %[a6], v[128:131], v[184:187], %[a6]\n"
        "ds_read_b128 v[240:243], %[fa] offset:1728\n"
        "v_mfma_f32_16x16x32_bf16 %[a7], v[128:131], v[188:191], %[a7]\n"
        "ds_read_b128 v[244:247], %[fa] offset:1216\n"
        "v_mfma_f32_16x16x32_bf16 %[a7], v[132:135], v[184:187], %[a7]\n"
        "ds_read_b128 v[248:251], %[fa] offset:704\n"
        "v_mfma_f32_16x16x32_bf16 %[a6], v[132:135], v[180:183], %[a6]\n"
        "ds_read_b128 v[252:255], %[fa] offset:192\n"
        "v_mfma_f32_16x16x32_bf16 %[a5], v[132:135], v[176:179], %[a5]\n"
        "ds_read_b64 v[196:197], %[ga] offset:704\n"
        "v_mfma_f32_16x16x32_bf16 %[a4], v[132:135], v[172:175], %[a4]\n"
        "ds_read_b64 v[198:199], %[gb] offset:704\n"
        "s_waitcnt lgkmcnt(15)\n"
        "v_mfma_f32_16x16x32_bf16 %[a7], v[136:139], v[180:183], %[a7]\n"
        "ds_read_b128 v[236:239], %[fa] offset:2240\n"
        "v_mfma_f32_16x16x32_bf16 %[a6], v[136:139], v[176:179], %[a6]\n"
        "ds_read_b64 v[200:201], %[ga] offset:1216\n"
        "v_mfma_f32_16x16x32_bf16 %[a5], v[136:139], v[172:175], %[a5]\n"
        "ds_read_b64 v[202:203], %[gb] offset:1216\n"
        "v_mfma_f32_16x16x32_bf16 %[a4], v[136:139], v[168:171], %[a4]\n"
        "ds_read_b128 v[232:235], %[fa] offset:2752\n"
        "v_mfma_f32_16x16x32_bf16 %[a7], v[140:143], v[176:179], %[a7]\n"
        "ds_read_b64 v[204:205], %[ga] offset:1728\n"
        "v_mfma_f32_16x16x32_bf16 %[a6], v[140:143], v[172:175], %[a6]\n"
        "ds_read_b64 v[206:207], %[gb] offset:1728\n"
        "v_mfma_f32_16x16x32_bf16 %[a5], v[140:143], v[168:171], %[a5]\n"
        "ds_read_b128 v[228:231], %[fa] offset:3264\n"
        "v_mfma_f32_16x16x32_bf16 %[a4], v[140:143], v[164:167], %[a4]\n"
        "ds_read_b64 v[208:209], %[ga] offset:2240\n"
        "v_mfma_f32_16x16x32_bf16 %[a7], v[144:147], v[172:175], %[a7]\n"
        "ds_read_b64 v[210:211], %[gb] offset:2240\n"
        "v_mfma_f32_16x16x32_bf16 %[a6], v[144:147], v[168:171], %[a6]\n"
        "ds_read_b128 v[224:227], %[fa] offset:3776\n"
        "v_mfma_f32_16x16x32_bf16 %[a5], v[144:147], v[164:167], %[a5]\n"
        "ds_read_b64 v[212:213], %[ga] offset:2752\n"
        "s_waitcnt lgkmcnt(15)\n"
        "v_mfma_f32_16x16x32_bf16 %[a4], v[144:147], v[160:163], %[a4]\n"
        "ds_read_b64 v[214:215], %[gb] offset:2752\n"
        "v_mfma_f32_16x16x32_bf16 %[a5], v[148:151], v[160:163], %[a5]\n"
        "ds_read_b64 v[216:217], %[ga] offset:3264\n"
        "v_mfma_f32_16x16x32_bf16 %[a6], v[148:151], v[164:167], %[a6]\n"
        "ds_read_b64 v[218:219], %[gb] offset:3264\n"
        "v_mfma_f32_16x16x32_bf16 %[a7], v[148:151], v[168:171], %[a7]\n"
        "ds_read_b64 v[220:221], %[ga] offset:3776\n"
        "v_mfma_f32_16x16x32_bf16 %[a6], v[152:155], v[160:163], %[a6]\n"
        "ds_read_b64 v[222:223], %[gb] offset:3776\n"
        "v_mfma_f32_16x16x32_bf16 %[a7], v[152:155], v[164:167], %[a7]\n"
        "v_mfma_f32_16x16x32_bf16 %[a7], v[156:159], v[160:163], %[a7]\n"
        "v_add_u32 %[ga], 128, %[ga]\n"
        "v_add_u32 %[gb], 128, %[gb]\n"
        "v_add_u32 %[fa], 128, %[fa]\n"
        "s_waitcnt lgkmcnt(8)\n"
        "s_sub_u32 %[cnt], %[cnt], 1\n"
        "s_cmp_lg_u32 %[cnt], 0\n"
        "s_cbranch_scc1 1b\n"
        "v_mfma_f32_16x16x32_bf16 %[a4], v[192:195], v[240:243], %[a4]\n"
        "v_mfma_f32_16x16x32_bf16 %[a5], v[192:195], v[244:247], %[a5]\n"
        "v_mfma_f32_16x16x32_bf16 %[a6], v[192:195], v[248:251], %[a6]\n"
        "v_mfma_f32_16x16x32_bf16 %[a7], v[192:195], v[252:255], %[a7]\n"
        "v_mfma_f32_16x16x32_bf16 %[a7], v[196:199], v[248:251], %[a7]\n"
        "v_mfma_f32_16x16x32_bf16 %[a6], v[196:199], v[244:247], %[a6]\n"
        "v_mfma_f32_16x16x32_bf16 %[a5], v[196:199], v[240:243], %[a5]\n"
        "v_mfma_f32_16x16x32_bf16 %[a4], v[196:199], v[236:239], %[a4]\n"
        "v_mfma_f32_16x16x32_bf16 %[a7], v[200:203], v[244:247], %[a7]\n"
        "v_mfma_f32_16x16x32_bf16 %[a6], v[200:203], v[240:243], %[a6]\n"
        "v_mfma_f32_16x16x32_bf16 %[a5], v[200:203], v[236:239], %[a5]\n"
        "v_mfma_f32_16x16x32_bf16 %[a4], v[200:203], v[232:235], %[a4]\n"
        "v_mfma_f32_16x16x32_bf16 %[a7], v[204:207], v[240:243], %[a7]\n"
        "v_mfma_f32_16x16x32_bf16 %[a6], v[204:207], v[236:239], %[a6]\n"
        "v_mfma_f32_16x16x32_bf16 %[a5], v[204:207], v[232:235], %[a5]\n"
        "v_mfma_f32_16x16x32_bf16 %[a4], v[204:207], v[228:231], %[a4]\n"
        "s_waitcnt lgkmcnt(7)\n"
        "v_mfma_f32_16x16x32_bf16 %[a7], v[208:211], v[236:239], %[a7]\n"
        "v_mfma_f32_16x16x32_bf16 %[a6], v[208:211], v[232:235], %[a6]\n"
        "v_mfma_f32_16x16x32_bf16 %[a5], v[208:211], v[228:231], %[a5]\n"
        "s_waitcnt lgkmcnt(6)\n"
        "v_mfma_f32_16x16x32_bf16 %[a4], v[208:211], v[224:227], %[a4]\n"
        "s_waitcnt lgkmcnt(4)\n"
        "v_mfma_f32_16x16x32_bf16 %[a5], v[212:215], v[224:227], %[a5]\n"
        "v_mfma_f32_16x16x32_bf16 %[a6], v[212:215], v[228:231], %[a6]\n"
        "v_mfma_f32_16x16x32_bf16 %[a7], v[212:215], v[232:235], %[a7]\n"
        "s_waitcnt lgkmcnt(2)\n"
        "v_mfma_f32_16x16x32_bf16 %[a6], v[216:219], v[224:227], %[a6]\n"
        "v_mfma_f32_16x16x32_bf16 %[a7], v[216:219], v[228:231], %[a7]\n"
        "s_waitcnt lgkmcnt(0)\n"
        "v_mfma_f32_16x16x32_bf16 %[a7], v[220:223], v[224:227], %[a7]\n"
        "s_setprio 0\n"
        "s_nop 7\n"
        "s_nop 1\n"
        : [ga] "+v"(ga), [gb] "+v"(gb), [fa] "+v"(fa), [a4] "=&v"(a4), [a5] "=&v"(a5), [a6] "=&v"(a6), [a7] "=&v"(a7), [cnt] "=&s"(cnt)
        : [ya] "v"(ya)
        : "memory", "scc", "v104", "v105", "v106", "v107", "v108", "v109", "v110", "v111", "v112", "v113", "v114", "v115", "v116", "v117", "v118", "v119", "v120", "v121", "v122", "v123", "v124", "v125", "v126", "v127", "v128", "v129", "v130", "v131", "v132", "v133", "v134", "v135", "v136", "v137", "v138", "v139", "v140", "v141", "v142", "v143", "v144", "v145", "v146", "v147", "v148", "v149", "v150", "v151", "v152", "v153", "v154", "v155", "v156", "v157", "v158", "v159", "v160", "v161", "v162", "v163", "v164", "v165", "v166", "v167", "v168", "v169", "v170", "v171", "v172", "v173", "v174", "v175", "v176", "v177", "v178", "v179", "v180", "v181", "v182", "v183", "v184", "v185", "v186", "v187", "v188", "v189", "v190", "v191", "v192", "v193", "v194", "v195", "v196", "v197", "v198", "v199", "v200", "v201", "v202", "v203", "v204", "v205", "v206", "v207", "v208", "v209", "v210", "v211", "v212", "v213", "v214", "v215", "v216", "v217", "v218", "v219", "v220", "v221", "v222", "v223", "v224", "v225", "v226", "v227", "v228", "v229", "v230", "v231", "v232", "v233", "v234", "v235", "v236", "v237", "v238", "v239", "v240", "v241", "v242", "v243", "v244", "v245", "v246", "v247", "v248", "v249", "v250", "v251", "v252", "v253", "v254", "v255");
}

__global__ void __launch_bounds__(NT, 2) fused_kernel(Params P) {
    extern __shared__ __attribute__((aligned(16))) char smem_[];
    const int tid = threadIdx.x, lane = tid & 63;
    const int wv = __builtin_amdgcn_readfirstlane(tid >> 6);
    const int w = blockIdx.x, grp = w & 7, ti = w >> 3;
    const int d0 = 64 * grp + 2 * ti;
    char* ws = P.ws;
    stamp(ws, 0);
    u32 pfv;
    {
        u64 pc; u32 len;
        asm volatile("s_getpc_b64 %0\n.Lfk_pf_start:\n\ts_mov_b32 %1, .Lfk_pf_end-.Lfk_pf_start" : "=s"(pc), "=s"(len));
        u32 off = (u32)tid * 64u;
        off = off < len - 4u ? off : len - 4u;
        pfv = *(const GAS u32*)(pc + (off & ~3u));
    }

    const __amdgpu_buffer_rsrc_t sig_rsrc = __builtin_amdgcn_make_buffer_rsrc(ws + WS_SIG, 0, 2 * D * S * 2, 0x00020000);
    const __amdgpu_buffer_rsrc_t g_rsrc = __builtin_amdgcn_make_buffer_rsrc(ws + WS_G, 0, S * 4, 0x00020000);

    const size_t toff = (size_t)(64 * ti + 8 * wv + (lane >> 3)) * D + 64 * grp + 8 * (lane & 7);
    const u32x4 lq = *(const u32x4*)(P.q + toff);
    const u32x4 lk = *(const u32x4*)(P.k + toff);
    const u32x4 lv = *(const u32x4*)(P.v + toff);
    const bool has_halo = (wv == 0) && (ti > 0);
    const u32x4 lh = *(const u32x4*)(P.k + (has_halo ? toff - 8 * D : toff));
    const u32x2 skw = *(const u32x2*)(P.sk + (64 * grp + 8 * wv + (lane >> 3)) * 4);
    const u32x4 gq = *(const u32x4*)(P.q + (size_t)(8 * w + wv) * D + 8 * lane);
    const u32x4 gwv = *(const u32x4*)(P.gw + 8 * lane);
    const int kch = wv >> 2;
    const int pidx = (d0 + kch) * NS + 64 * ((wv >> 1) & 1) + lane;
    const u16 pa = P.are[pidx], pb = P.aim[pidx], pc = P.bre[pidx], pd = P.bim[pidx], pe = P.cre[pidx], pf = P.cim[pidx];
    const u16 pl = P.ldt[d0 + kch];
    const u16* ssrc = P.ssm + (size_t)(d0 + (tid >> 8)) * S + 8 * (tid & 255);
    const u32x4 sm = *(const u32x4*)ssrc;
    const u32x2 sm2 = *(const u32x2*)(ssrc + ((tid & 255) < 255 ? 8 : 0));
    const u64 tag = *(const u64*)(ws + WS_EPOCH) + 1;

    int gidx[11];
    {
        const int ch = tid >> 8, j = tid & 255;
#pragma unroll
        for (int m = 0; m < 11; ++m) {
            const int l = 8 * j + m;
            const int bits = (int)(pk(0.f, (float)l) >> 16);
            int idx = l < 128 ? l : bits - 0x4300 + 128;
            idx = idx > 640 ? 640 : idx;
            gidx[m] = L_KB + ch * 1312 + 2 * idx;
            asm volatile("" : "+v"(gidx[m]));
        }
    }
    if (tid == 0) lds_st<u32>(L_MISC, 0u);
    if (tid < 4 * 30) lds_st<u32x4>(L_SIG + (tid / 30) * SIG_ST + 4096 + (tid % 30) * 16, (u32x4){0, 0, 0, 0});

    {
        const int r = lane >> 3, o = lane & 7;
        const u32 sel = (r & 1) ? 0x07060302u : 0x01000504u;
        const int c = 8 * o + r;
        const int a = L_T + c * 128 + (((7 - wv) ^ o) << 4);
        lds_st<u32x4>(a + 8192, tr8(lk, sel));
        lds_st<u32x4>(a + 16384, tr8(lv, sel));
        lds_st<u32x4>(a, tr8(lq, sel));
        if (wv == 0) { u32x4 h = tr8(lh, sel); if (!has_halo) h = (u32x4){0, 0, 0, 0}; lds_st<u32x4>(L_H + c * 16, h); }
    }
    stamp(ws, 1);
    __syncthreads();

    {
        const int cc = lane >> 3, oct = lane & 7, c = 8 * wv + cc;
        const int a = L_T + c * 128 + ((oct ^ wv) << 4);
        const u32x4 tk = lds_ld<u32x4>(a + 8192);
        const int an = oct < 7 ? (L_T + 8192 + c * 128 + (((oct + 1) ^ wv) << 4)) : (L_H + c * 16);
        const u32x2 tn = lds_ld<u32x2>(an);
        const u32x4 tv = lds_ld<u32x4>(a + 16384), tq = lds_ld<u32x4>(a);
        const u32 go = ((64 * grp + c) * S + 1984 - 64 * ti + 8 * oct) * 2;
        float kk[11];
#pragma unroll
        for (int m = 0; m < 4; ++m) { kk[2 * m] = lo2f(tk[m]); kk[2 * m + 1] = hi2f(tk[m]); }
        kk[8] = lo2f(tn[0]); kk[9] = hi2f(tn[0]); kk[10] = lo2f(tn[1]);
        const float s0 = lo2f(skw[0]), s1 = hi2f(skw[0]), s2 = lo2f(skw[1]), s3 = hi2f(skw[1]);
        float kv[8];
#pragma unroll
        for (int p = 0; p < 8; ++p) {
            const float acc = ffma(kk[p], s0, ffma(kk[p + 1], s1, ffma(kk[p + 2], s2, fmul(kk[p + 3], s3))));
            kv[p] = fmul(acc, (p & 1) ? hi2f(tv[p >> 1]) : lo2f(tv[p >> 1]));
        }
        const u32x4 okv = {pk(kv[0], kv[1]), pk(kv[2], kv[3]), pk(kv[4], kv[5]), pk(kv[6], kv[7])};
        __builtin_amdgcn_raw_buffer_store_b128(okv, sig_rsrc, go, 0, 16);
        __builtin_amdgcn_raw_buffer_store_b128(tq, sig_rsrc, go + D * S * 2, 0, 16);
    }
    stamp(ws, 2);
    {
        float z = 0.f;
#pragma unroll
        for (int m = 0; m < 4; ++m) { z += rb(lo2f(gq[m]) * lo2f(gwv[m])); z += rb(hi2f(gq[m]) * hi2f(gwv[m])); }
        z += __uint_as_float(__builtin_amdgcn_update_dpp(0u, __float_as_uint(z), 0x128, 0xf, 0xf, true));
        z += __uint_as_float(__builtin_amdgcn_update_dpp(0u, __float_as_uint(z), 0x124, 0xf, 0xf, true));
        z += __uint_as_float(__builtin_amdgcn_update_dpp(0u, __float_as_uint(z), 0x122, 0xf, 0xf, true));
        z += __uint_as_float(__builtin_amdgcn_update_dpp(0u, __float_as_uint(z), 0x121, 0xf, 0xf, true));
        { auto s = __builtin_amdgcn_permlane16_swap(__float_as_uint(z), __float_as_uint(z), false, false); z = __uint_as_float(s[0]) + __uint_as_float(s[1]); }
        { auto s = __builtin_amdgcn_permlane32_swap(__float_as_uint(z), __float_as_uint(z), false, false); z = __uint_as_float(s[0]) + __uint_as_float(s[1]); }
        z = rb(z);
        const float g = __builtin_amdgcn_rcpf(1.f + __builtin_amdgcn_exp2f(z * -1.44269504088896341f));
        if (lane == 0) __builtin_amdgcn_raw_buffer_store_b32(pk(g, 0.f), g_rsrc, 4 * (8 * w + wv), 0, 16);
    }

    {
        const float dt = rb(expf(bf2f(pl)));
        const float la = dt * bf2f(pa) * 1.44269504088896341f;
        float rv = dt * bf2f(pb) * 0.15915494309189535f;
        rv -= rintf(rv);
        const u32 tb = L_X + kch * TB_CH + 256 * ((wv >> 1) & 1);
        const cpx w128 = cexp_rev(la * 128.f, rv * 128.f);
        const cpx w256 = cmul(w128, w128);
        if (wv & 1) {
            const cpx w1 = cexp_rev(la, rv);
            const cpx w2 = cmul(w1, w1), w3 = cmul(w2, w1), w4 = cmul(w2, w2), w5 = cmul(w4, w1), w6 = cmul(w4, w2), w7 = cmul(w4, w3);
            st4_addtid<0 * TB_ROW, 1 * TB_ROW, 2 * TB_ROW, 3 * TB_ROW>(tb, 0x3f80u, pkc(w1), pkc(w2), pkc(w3));
            st4_addtid<4 * TB_ROW, 5 * TB_ROW, 6 * TB_ROW, 7 * TB_ROW>(tb, pkc(w4), pkc(w5), pkc(w6), pkc(w7));
            st4_addtid<8 * TB_ROW, 9 * TB_ROW, 10 * TB_ROW, 11 * TB_ROW>(tb, pkc(w128), pkc(cmul(w128, w1)), pkc(cmul(w128, w2)), pkc(cmul(w128, w3)));
            st4_addtid<12 * TB_ROW, 13 * TB_ROW, 14 * TB_ROW, 15 * TB_ROW>(tb, pkc(cmul(w128, w4)), pkc(cmul(w128, w5)), pkc(cmul(w128, w6)), pkc(cmul(w128, w7)));
            const cpx w384 = cmul(w256, w128), w512 = cmul(w256, w256);
            st4_addtid<16 * TB_ROW, 17 * TB_ROW, 18 * TB_ROW, 19 * TB_ROW>(tb, pkc(w256), pkc(cmul(w256, w2)), pkc(cmul(w256, w4)), pkc(cmul(w256, w6)));
            st4_addtid<20 * TB_ROW, 21 * TB_ROW, 22 * TB_ROW, 23 * TB_ROW>(tb, pkc(w384), pkc(cmul(w384, w2)), pkc(cmul(w384, w4)), pkc(cmul(w384, w6)));
            const cpx z1 = cmul(w512, w128), z2 = cmul(w512, w256), z3 = cmul(w512, w384);
            st4_addtid<24 * TB_ROW, 25 * TB_ROW, 26 * TB_ROW, 27 * TB_ROW>(tb, pkc(w512), pkc(cmul(w512, w4)), pkc(z1), pkc(cmul(z1, w4)));
            st4_addtid<28 * TB_ROW, 29 * TB_ROW, 30 * TB_ROW, 31 * TB_ROW>(tb, pkc(z2), pkc(cmul(z2, w4)), pkc(z3), pkc(cmul(z3, w4)));
        } else {
            const float br = dt * bf2f(pc), bi = dt * bf2f(pd), cr = bf2f(pe), ci = bf2f(pf);
            cpx Q[16];
            Q[0] = (cpx){cr * br - ci * bi, -(cr * bi + ci * br)};
            const cpx u1 = cexp_rev(la * 8.f, rv * -8.f), u2 = cmul(u1, u1), u4 = cmul(u2, u2), u8 = cmul(u4, u4);
            Q[1] = cmul(Q[0], u1); Q[2] = cmul(Q[0], u2); Q[3] = cmul(Q[1], u2);
            st4_addtid<48 * TB_ROW, 49 * TB_ROW, 50 * TB_ROW, 51 * TB_ROW>(tb, pkc(Q[0]), pkc(Q[1]), pkc(Q[2]), pkc(Q[3]));
#pragma unroll
            for (int b = 0; b < 4; ++b) Q[4 + b] = cmul(Q[b], u4);
            st4_addtid<52 * TB_ROW, 53 * TB_ROW, 54 * TB_ROW, 55 * TB_ROW>(tb, pkc(Q[4]), pkc(Q[5]), pkc(Q[6]), pkc(Q[7]));
            st4_addtid<56 * TB_ROW, 57 * TB_ROW, 58 * TB_ROW, 59 * TB_ROW>(tb, pkc(cmul(Q[0], u8)), pkc(cmul(Q[1], u8)), pkc(cmul(Q[2], u8)), pkc(cmul(Q[3], u8)));
            st4_addtid<60 * TB_ROW, 61 * TB_ROW, 62 * TB_ROW, 63 * TB_ROW>(tb, pkc(cmul(Q[4], u8)), pkc(cmul(Q[5], u8)), pkc(cmul(Q[6], u8)), pkc(cmul(Q[7], u8)));
            const cpx R0 = cexp_rev(la * 1024.f, __builtin_amdgcn_fractf(rv * 1024.f));
            const cpx w512 = cmul(w256, w256);
            const cpx R1 = cmul(R0, w128), R2 = cmul(R0, w256), R3 = cmul(R1, w256);
            st4_addtid<32 * TB_ROW, 33 * TB_ROW, 34 * TB_ROW, 35 * TB_ROW>(tb, pkc(R0), pkc(R1), pkc(R2), pkc(R3));
            st4_addtid<36 * TB_ROW, 37 * TB_ROW, 38 * TB_ROW, 39 * TB_ROW>(tb, pkc(cmul(R0, w512)), pkc(cmul(R1, w512)), pkc(cmul(R2, w512)), pkc(cmul(R3, w512)));
            lds_st<u32>(tb + 40 * TB_ROW + 4 * lane, pkc(cmul(R0, R0)));
        }
    }
    stamp(ws, 3);
    write_copies(L_KTA + (tid >> 8) * KT_CC, tid & 255, sm[0], sm[1], sm[2], sm[3], sm2[0], (tid & 255) < 255 ? sm2[1] : 0u);
    {
        asm volatile("s_waitcnt vmcnt(0)" ::: "memory");
        u32 old = 0;
        if (lane == 0) old = __hip_atomic_fetch_add((LAS u32*)(size_t)L_MISC, 1u, __ATOMIC_RELAXED, __HIP_MEMORY_SCOPE_WORKGROUP);
        old = __builtin_amdgcn_readfirstlane(old);
        if (old == 7 && lane == 0) __hip_atomic_store((gu64*)(ws + WS_FLAG1) + grp * 32 + ti, tag, __ATOMIC_RELAXED, __HIP_MEMORY_SCOPE_AGENT);
    }
    stamp(ws, 4);
    __syncthreads();
    stamp(ws, 5);
    if (wv < 6) {
        const int ch = wv >= 3, rt = wv - 3 * ch, r = lane & 15, q = lane >> 4;
        const int ab = L_X + ch * TB_CH + (16 * rt + r) * TB_ROW + 16 * q, bb = L_X + ch * TB_CH + (48 + r) * TB_ROW + 16 * q;
        u32x4 A[8], B[8];
#pragma unroll
        for (int t = 0; t < 8; ++t) { A[t] = lds_ld<u32x4>(ab + 64 * t); B[t] = lds_ld<u32x4>(bb + 64 * t); }
        int ix[4];
#pragma unroll
        for (int i = 0; i < 4; ++i) {
            const int row = 4 * q + i, b = r;
            int idx;
            if (rt == 0) idx = (row < 8 ? row : 120 + row) + 8 * b;
            else if (rt == 1) idx = row < 8 ? 256 + 64 * (row >> 2) + (row & 3) + 4 * b : 384 + 32 * ((row - 8) >> 1) + (row & 1) + 2 * b;
            else idx = row < 8 ? 512 + 16 * row + b : ((row == 8 && b == 0) ? 640 : 648);
            ix[i] = L_KB + ch * 1312 + 2 * idx;
        }
        f32x4 acc0 = {0, 0, 0, 0}, acc1 = {0, 0, 0, 0};
#pragma unroll
        for (int t = 0; t < 8; t += 2) {
            acc0 = __builtin_amdgcn_mfma_f32_16x16x32_bf16(__builtin_bit_cast(bf16x8, A[t]), __builtin_bit_cast(bf16x8, B[t]), acc0, 0, 0, 0);
            acc1 = __builtin_amdgcn_mfma_f32_16x16x32_bf16(__builtin_bit_cast(bf16x8, A[t + 1]), __builtin_bit_cast(bf16x8, B[t + 1]), acc1, 0, 0, 0);
        }
#pragma unroll
        for (int i = 0; i < 4; ++i) lds_st<u16>(ix[i], (u16)(pk(acc0[i] + acc1[i], 0.f) & 0xffffu));
    }
    __syncthreads();
    {
        u32 e[11];
#pragma unroll
        for (int m = 0; m < 11; ++m) e[m] = lds_ld<u16>(gidx[m]);
        write_copies(L_X + (tid >> 8) * KT_CC, tid & 255, e[0] | (e[1] << 16), e[2] | (e[3] << 16), e[4] | (e[5] << 16), e[6] | (e[7] << 16), e[8] | (e[9] << 16), e[10]);
    }
    stamp(ws, 6);

    wait_flags((const gu64*)(ws + WS_FLAG1) + grp * 32 + (lane & 31), tag);
    stamp(ws, 7);
    {
        const int sg = wv >> 1, half = wv & 1;
        const u32 go = (((sg & 1) * D + d0 + (sg >> 1)) * S) * 2 + half * 2048 + lane * 16;
        const u32x4 v0 = __builtin_amdgcn_raw_buffer_load_b128(sig_rsrc, go, 0, 16);
        const u32x4 v1 = __builtin_amdgcn_raw_buffer_load_b128(sig_rsrc, go + 1024, 0, 16);
        const int la = L_SIG + sg * SIG_ST + half * 2048 + lane * 16;
        lds_st<u32x4>(la, v0);
        lds_st<u32x4>(la + 1024, v1);
    }
    __syncthreads();
    stamp(ws, 8);

    if (wv < 4) {
        const int r = lane & 15, q = lane >> 4;
        const int a = (r + 1) & 3;
        const int ga = ((wv & 1) ? L_X : L_KTA) + (wv >> 1) * KT_CC + a * KT_COPY + 2 * (1 + r + 8 * q - a);
        const int fa = L_SIG + wv * SIG_ST + 2 * (240 - 16 * r + 8 * q);
        const int ya = L_Y + wv * 4096 + 2 * (16 * r + 4 * q);
        f32x4 acc[4];
        conv_all(ga, ga + 8, fa, ya, acc[0], acc[1], acc[2], acc[3]);
        stamp(ws, 9);
#pragma unroll
        for (int ct = 0; ct < 4; ++ct) lds_st<u32x2>(ya + 512 * (4 + ct), (u32x2){pk(acc[ct][0], acc[ct][1]), pk(acc[ct][2], acc[ct][3])});
        __syncthreads();
    } else {
        {
            const gu64* f = (const gu64*)(ws + WS_FLAG1) + lane;
            for (u32 spins = 0; spins < (1u << 18); ++spins) {
                const u64 x0 = __hip_atomic_load(f, __ATOMIC_RELAXED, __HIP_MEMORY_SCOPE_AGENT), x1 = __hip_atomic_load(f + 64, __ATOMIC_RELAXED, __HIP_MEMORY_SCOPE_AGENT);
                const u64 x2 = __hip_atomic_load(f + 128, __ATOMIC_RELAXED, __HIP_MEMORY_SCOPE_AGENT), x3 = __hip_atomic_load(f + 192, __ATOMIC_RELAXED, __HIP_MEMORY_SCOPE_AGENT);
                if (__all(x0 == tag && x1 == tag && x2 == tag && x3 == tag)) break;
                __builtin_amdgcn_s_sleep(1);
            }
        }
        if (w == 0 && tid == 256) __hip_atomic_store((gu64*)(ws + WS_EPOCH), tag, __ATOMIC_RELAXED, __HIP_MEMORY_SCOPE_AGENT);
        const int t4 = 4 * (tid - 256);
        const u32x4 gg0 = __builtin_amdgcn_raw_buffer_load_b128(g_rsrc, 4 * t4, 0, 16), gg1 = __builtin_amdgcn_raw_buffer_load_b128(g_rsrc, 4 * t4 + 4096, 0, 16);
        const float sD0 = bf2f(P.sD[d0]), sD1 = bf2f(P.sD[d0 + 1]), dk0 = bf2f(P.dsk[d0]), dk1 = bf2f(P.dsk[d0 + 1]);
        float cA[2][2][4], cB[2][4], c0[2][2][4];
#pragma unroll
        for (int h = 0; h < 2; ++h) {
            const int s0 = t4 + 1024 * h;
#pragma unroll
            for (int ch = 0; ch < 2; ++ch) {
                const u32x2 kv = lds_ld<u32x2>(L_SIG + (2 * ch) * SIG_ST + 2 * (2044 - s0)), x = lds_ld<u32x2>(L_SIG + (2 * ch + 1) * SIG_ST + 2 * (2044 - s0));
                const float sD = ch ? sD1 : sD0, dk = ch ? dk1 : dk0;
#pragma unroll
                for (int i = 0; i < 4; ++i) {
                    const int ri = 3 - i;
                    const float fkv = (ri & 1) ? hi2f(kv[ri >> 1]) : lo2f(kv[ri >> 1]), fx = (ri & 1) ? hi2f(x[ri >> 1]) : lo2f(x[ri >> 1]);
                    const float g = lo2f(h ? gg1[i] : gg0[i]);
                    const float omg = rb(1.f - g), gx = g * fx;
                    cA[h][ch][i] = gx; cB[h][i] = omg;
                    c0[h][ch][i] = gx * (fkv * sD) + omg * (fx * dk);
                    asm volatile("" : "+v"(cA[h][ch][i]), "+v"(c0[h][ch][i]), "+v"(cB[h][i]));
                }
            }
        }
        stamp(ws, 9);
#pragma unroll
        for (int h = 0; h < 2; ++h) {
            __syncthreads();
            if (h == 0) stamp(ws, 12);
            const int s0 = t4 + 1024 * h;
            float res[2][4];
#pragma unroll
            for (int ch = 0; ch < 2; ++ch) {
                const u32x2 ya = lds_ld<u32x2>(L_Y + (2 * ch) * 4096 + 2 * s0), yb = lds_ld<u32x2>(L_Y + (2 * ch + 1) * 4096 + 2 * s0);
#pragma unroll
                for (int i = 0; i < 4; ++i) {
                    const float fya = (i & 1) ? hi2f(ya[i >> 1]) : lo2f(ya[i >> 1]), fyb = (i & 1) ? hi2f(yb[i >> 1]) : lo2f(yb[i >> 1]);
                    res[ch][i] = ffma(cA[h][ch][i], fya, ffma(cB[h][i], fyb, c0[h][ch][i]));
                }
            }
#pragma unroll
            for (int i = 0; i < 4; ++i) *(u32*)(P.out + (size_t)(s0 + i) * D + d0) = pk(res[0][i], res[1][i]);
            if (h == 0) stamp(ws, 10);
        }
    }
    stamp(ws, 11);
    asm volatile(".Lfk_pf_end:" :: "v"(pfv));
}

extern "C" void kernel_launch(void* const* d_in, const int* in_sizes, int n_in, void* d_out, int out_size, void* d_ws, size_t ws_size, hipStream_t stream) {
    Params p;
    p.q = (const u16*)d_in[0]; p.k = (const u16*)d_in[1]; p.v = (const u16*)d_in[2]; p.sk = (const u16*)d_in[3];
    p.ssm = (const u16*)d_in[4]; p.sD = (const u16*)d_in[5];
    p.are = (const u16*)d_in[7]; p.aim = (const u16*)d_in[8]; p.bre = (const u16*)d_in[9]; p.bim = (const u16*)d_in[10];
    p.cre = (const u16*)d_in[11]; p.cim = (const u16*)d_in[12]; p.ldt = (const u16*)d_in[13]; p.dsk = (const u16*)d_in[14]; p.gw = (const u16*)d_in[15];
    p.out = (u16*)d_out; p.ws = (char*)d_ws;
    (void)hipFuncSetAttribute((const void*)fused_kernel, hipFuncAttributeMaxDynamicSharedMemorySize, L_END);
    fused_kernel<<<NWG, NT, L_END, stream>>>(p);
}
```

```cpp
#include <hip/hip_runtime.h>
#include <stdint.h>

typedef unsigned short u16;
typedef unsigned int u32;
typedef unsigned long long u64;
typedef u32 u32x4 __attribute__((ext_vector_type(4)));
typedef u32 u32x2 __attribute__((ext_vector_type(2)));
typedef float f32x4 __attribute__((ext_vector_type(4)));
typedef float f32x2 __attribute__((ext_vector_type(2)));
typedef __bf16 bf16x2 __attribute__((ext_vector_type(2)));
typedef __bf16 bf16x8 __attribute__((ext_vector_type(8)));
#define GAS __attribute__((address_space(1)))
#define LAS __attribute__((address_space(3)))
typedef GAS u64 gu64;

#define FSTR 16
#define POLL_DELAY 2
#define POLL_GAP 1
#define STAMPS 0

constexpr int S = 2048, D = 512, NS = 128;
constexpr int NWG = 256, NT = 512;

constexpr size_t WS_EPOCH = 0;
constexpr size_t WS_FLAG1 = 4096;
constexpr size_t WS_G     = 49152;
constexpr size_t WS_SIG   = 65536;
constexpr size_t WS_STAMP = 8u << 20;

constexpr int KT_COPY = 4160;
constexpr int KT_CC   = 4 * KT_COPY;
constexpr int TB_ROW  = 528, TB_CH = 64 * TB_ROW;
constexpr int L_X     = 0;
constexpr int L_Y     = L_X + 2 * KT_CC;
constexpr int L_KTA   = L_X + 2 * TB_CH;
constexpr int SIG_ST  = 4608;
constexpr int L_SIG   = L_KTA + 2 * KT_CC;
constexpr int L_T     = L_SIG + 4 * SIG_ST;
constexpr int L_H     = L_T + 3 * 8192;
constexpr int L_KB    = L_H + 1024;
constexpr int L_MISC  = L_KB + 2 * 1312;
constexpr int L_END   = L_MISC + 64;
static_assert(L_Y + 16384 <= L_KTA, "Y overlay");

__device__ __forceinline__ u32 pk(float lo, float hi) { f32x2 v = {lo, hi}; bf16x2 b = __builtin_convertvector(v, bf16x2); return __builtin_bit_cast(u32, b); }
__device__ __forceinline__ float rb(float x) { return __uint_as_float(pk(0.f, x)); }
__device__ __forceinline__ float lo2f(u32 d) { return __uint_as_float(d << 16); }
__device__ __forceinline__ float hi2f(u32 d) { return __uint_as_float(d & 0xffff0000u); }
__device__ __forceinline__ float bf2f(u16 v) { return __uint_as_float(((u32)v) << 16); }

template <typename T> __device__ __forceinline__ T lds_ld(int off) { return *(const LAS T*)(size_t)(u32)off; }
template <typename T> __device__ __forceinline__ void lds_st(int off, T v) { *(LAS T*)(size_t)(u32)off = v; }

struct IdxTabs { u16 g[256][12]; u16 x[3][64][4]; };
constexpr int bf16_round_int(int l) {
    int sh = 0; while ((l >> sh) >= 256) ++sh;
    if (sh == 0) return l;
    const int q = l >> sh, rem = l & ((1 << sh) - 1), half = 1 << (sh - 1);
    const int up = rem > half || (rem == half && (q & 1));
    return (q + up) << sh;
}
constexpr int kt_index(int l) {
    const int v = bf16_round_int(l);
    return v < 256 ? v : v < 512 ? 256 + (v - 256) / 2 : v < 1024 ? 384 + (v - 512) / 4 : 512 + (v - 1024) / 8;
}
constexpr IdxTabs make_tabs() {
    IdxTabs t{};
    for (int j = 0; j < 256; ++j)
        for (int m = 0; m < 12; ++m) { int l = 8 * j + m; if (l > 2047) l = 2047; t.g[j][m] = (u16)(2 * kt_index(l)); }
    for (int rt = 0; rt < 3; ++rt)
        for (int lane = 0; lane < 64; ++lane)
            for (int i = 0; i < 4; ++i) {
                const int row = 4 * (lane >> 4) + i, b = lane & 15;
                int idx = 0;
                if (rt == 0) idx = (row < 8 ? row : 120 + row) + 8 * b;
                else if (rt == 1) idx = row < 8 ? 256 + 64 * (row >> 2) + (row & 3) + 4 * b : 384 + 32 * ((row - 8) >> 1) + (row & 1) + 2 * b;
                else idx = row < 8 ? 512 + 16 * row + b : ((row == 8 && b == 0) ? 640 : 648);
                t.x[rt][lane][i] = (u16)(2 * idx);
            }
    return t;
}
__device__ static const IdxTabs idx_tabs = make_tabs();

struct Params {
    const u16 *q, *k, *v, *sk, *ssm, *sD, *are, *aim, *bre, *bim, *cre, *cim, *ldt, *dsk, *gw;
    u16* out; char* ws;
};

#define stamp(ws, slot) do {} while (0)
#define STAMP_DECL
#define STAMP_FLUSH do {} while (0)

template <int N>
__device__ __forceinline__ void tr8n(u32x4 (&d)[N], u32 sel) {
    u32 e[N][4];
#pragma unroll
    for (int n = 0; n < N; ++n)
#pragma unroll
        for (int m = 0; m < 4; ++m) e[n][m] = __builtin_amdgcn_update_dpp(0u, d[n][m], 0x128, 0xf, 0xf, true);
#pragma unroll
    for (int n = 0; n < N; ++n)
#pragma unroll
        for (int m = 0; m < 4; ++m) e[n][m] = __builtin_amdgcn_perm(e[n][m], d[n][m], sel);
#pragma unroll
    for (int n = 0; n < N; ++n) {
        auto a = __builtin_amdgcn_permlane16_swap(e[n][0], e[n][1], false, false);
        auto b = __builtin_amdgcn_permlane16_swap(e[n][2], e[n][3], false, false);
        e[n][0] = a[0]; e[n][1] = a[1]; e[n][2] = b[0]; e[n][3] = b[1];
    }
#pragma unroll
    for (int n = 0; n < N; ++n) {
        auto c = __builtin_amdgcn_permlane32_swap(e[n][0], e[n][2], false, false);
        auto f = __builtin_amdgcn_permlane32_swap(e[n][1], e[n][3], false, false);
        d[n] = (u32x4){f[1], c[1], f[0], c[0]};
    }
}

__device__ __forceinline__ void write_copies(int base, int j, u32 d0, u32 d1, u32 d2, u32 d3, u32 d4, u32 d5) {
    u32 a10 = __builtin_amdgcn_alignbit(d1, d0, 16), a21 = __builtin_amdgcn_alignbit(d2, d1, 16), a32 = __builtin_amdgcn_alignbit(d3, d2, 16),
        a43 = __builtin_amdgcn_alignbit(d4, d3, 16), a54 = __builtin_amdgcn_alignbit(d5, d4, 16);
    int o = base + 32 + 16 * j;
    lds_st<u32x4>(o, (u32x4){d0, d1, d2, d3});
    lds_st<u32x4>(o + KT_COPY, (u32x4){a10, a21, a32, a43});
    lds_st<u32x4>(o + 2 * KT_COPY, (u32x4){d1, d2, d3, d4});
    lds_st<u32x4>(o + 3 * KT_COPY, (u32x4){a21, a32, a43, a54});
}
__device__ __forceinline__ void write_edges(int base, int lane, u32 k01, u32 k23) {
    const int a = lane & 3, ch = (lane >> 2) & 1, z = lane >> 3;
    u32 d2 = a == 3 ? k01 << 16 : 0u;
    u32 d3 = a == 0 ? 0u : a == 1 ? k01 << 16 : a == 2 ? k01 : __builtin_amdgcn_alignbit(k23, k01, 16);
    if (z) { d2 = 0u; d3 = 0u; }
    if (lane < 16) lds_st<u32x4>(base + ch * KT_CC + a * KT_COPY + (z ? 0 : 16), (u32x4){0u, 0u, d2, d3});
}
__device__ __forceinline__ void lds_arrive(int addr, int lane) {
    asm volatile("" ::: "memory");
    if (lane == 0) __hip_atomic_fetch_add((LAS u32*)(size_t)(u32)addr, 1u, __ATOMIC_RELAXED, __HIP_MEMORY_SCOPE_WORKGROUP);
}
__device__ __forceinline__ void lds_await(int addr, u32 n) {
#pragma unroll 1
    for (u32 spins = 0; spins < (1u << 24); ++spins)
        if (*(const volatile LAS u32*)(size_t)(u32)addr >= n) break;
    asm volatile("" ::: "memory");
}

__device__ __forceinline__ bool wait_flags(const gu64* f, u64 tag) {
#pragma unroll 1
    for (u32 spins = 0; spins < (1u << 18); ++spins) {
        u64 x = __hip_atomic_load(f, __ATOMIC_RELAXED, __HIP_MEMORY_SCOPE_AGENT);
        if (__all(x == tag)) return true;
        __builtin_amdgcn_s_sleep(POLL_GAP);
    }
    return false;
}

typedef f32x2 cpx;
__device__ __forceinline__ cpx cmul(cpx a, cpx b) {
    cpx r;
    asm("v_pk_mul_f32 %0, %1, %2 op_sel_hi:[0,1]\n\tv_pk_fma_f32 %0, %1, %2, %0 op_sel:[1,1,0] op_sel_hi:[1,0,1] neg_lo:[1,0,0]" : "=&v"(r) : "v"(a), "v"(b));
    return r;
}
__device__ __forceinline__ cpx cexp_rev(float lm, float rev) {
    const float e = __builtin_amdgcn_exp2f(lm);
    return (cpx){e * __builtin_amdgcn_cosf(rev), e * __builtin_amdgcn_sinf(rev)};
}
__device__ __forceinline__ u32 pkc(cpx a) { return pk(a.x, a.y); }
__device__ __forceinline__ float ffma(float a, float b, float c) { float r; asm("v_fma_f32 %0, %1, %2, %3" : "=v"(r) : "v"(a), "v"(b), "v"(c)); return r; }
__device__ __forceinline__ float fmul(float a, float b) { float r; asm("v_mul_f32 %0, %1, %2" : "=v"(r) : "v"(a), "v"(b)); return r; }
template <int O0, int O1, int O2, int O3>
__device__ __forceinline__ void st4_addtid(u32 base, u32 a, u32 b, u32 c, u32 d) {
    u32 keep;
    asm volatile("s_mov_b32 %0, m0\n\ts_mov_b32 m0, %1\n\ts_nop 0\n\t"
                 "ds_write_addtid_b32 %2 offset:%6\n\tds_write_addtid_b32 %3 offset:%7\n\tds_write_addtid_b32 %4 offset:%8\n\tds_write_addtid_b32 %5 offset:%9\n\t"
                 "s_mov_b32 m0, %0"
                 : "=&s"(keep) : "s"(base), "v"(a), "v"(b), "v"(c), "v"(d), "i"(O0), "i"(O1), "i"(O2), "i"(O3) : "memory");
}

__device__ __forceinline__ void conv_all(int ga, int gb, int fa, int ya, int yf, f32x4& a4, f32x4& a5, f32x4& a6, f32x4& a7) {
    u32 cnt;
    asm volatile(
        "s_setprio 3\n"
        "ds_read_b64 v[128:129], %[ga] offset:0\n"
        "ds_read_b64 v[130:131], %[gb] offset:0\n"
        "ds_read_b128 v[144:147], %[fa] offset:3584\n"
        "ds_read_b128 v[148:151], %[fa] offset:3072\n"
        "ds_read_b128 v[152:155], %[fa] offset:2560\n"
        "ds_read_b128 v[156:159], %[fa] offset:2048\n"
        "ds_read_b64 v[132:133], %[ga] offset:512\n"
        "ds_read_b64 v[134:135], %[gb] offset:512\n"
        "ds_read_b64 v[136:137], %[ga] offset:1024\n"
        "ds_read_b64 v[138:139], %[gb] offset:1024\n"
        "ds_read_b64 v[140:141], %[ga] offset:1536\n"
        "ds_read_b64 v[142:143], %[gb] offset:1536\n"
        "ds_read_b64 v[160:161], %[ga] offset:64\n"
        "ds_read_b64 v[162:163], %[gb] offset:64\n"
        "ds_read_b128 v[176:179], %[fa] offset:3648\n"
        "ds_read_b128 v[180:183], %[fa] offset:3136\n"
        "ds_read_b128 v[184:187], %[fa] offset:2624\n"
        "ds_read_b128 v[188:191], %[fa] offset:2112\n"
        "ds_read_b64 v[164:165], %[ga] offset:576\n"
        "ds_read_b64 v[166:167], %[gb] offset:576\n"
        "ds_read_b64 v[168:169], %[ga] offset:1088\n"
        "ds_read_b64 v[170:171], %[gb] offset:1088\n"
        "ds_read_b64 v[172:173], %[ga] offset:1600\n"
        "ds_read_b64 v[174:175], %[gb] offset:1600\n"
        "s_waitcnt lgkmcnt(15)\n"
        "v_mfma_f32_16x16x32_bf16 v[112:115], v[128:131], v[144:147], 0\n"
        "ds_read_b64 v[192:193], %[ga] offset:128\n"
        "ds_read_b64 v[194:195], %[gb] offset:128\n"
        "v_mfma_f32_16x16x32_bf16 v[116:119], v[128:131], v[148:151], 0\n"
        "ds_read_b128 v[208:211], %[fa] offset:3712\n"
        "v_mfma_f32_16x16x32_bf16 v[120:123], v[128:131], v[152:155], 0\n"
        "ds_read_b128 v[212:215], %[fa] offset:3200\n"
        "v_mfma_f32_16x16x32_bf16 v[124:127], v[128:131], v[156:159], 0\n"
        "ds_read_b128 v[216:219], %[fa] offset:2688\n"
        "ds_read_b128 v[220:223], %[fa] offset:2176\n"
        "v_mfma_f32_16x16x32_bf16 v[116:119], v[132:135], v[144:147], v[116:119]\n"
        "ds_read_b64 v[196:197], %[ga] offset:640\n"
        "v_mfma_f32_16x16x32_bf16 v[120:123], v[132:135], v[148:151], v[120:123]\n"
        "ds_read_b64 v[198:199], %[gb] offset:640\n"
        "v_mfma_f32_16x16x32_bf16 v[124:127], v[132:135], v[152:155], v[124:127]\n"
        "ds_read_b64 v[200:201], %[ga] offset:1152\n"
        "ds_read_b64 v[202:203], %[gb] offset:1152\n"
        "s_waitcnt lgkmcnt(15)\n"
        "v_mfma_f32_16x16x32_bf16 v[120:123], v[136:139], v[144:147], v[120:123]\n"
        "ds_read_b64 v[204:205], %[ga] offset:1664\n"
        "v_mfma_f32_16x16x32_bf16 v[124:127], v[136:139], v[148:151], v[124:127]\n"
        "ds_read_b64 v[206:207], %[gb] offset:1664\n"
        "v_mfma_f32_16x16x32_bf16 v[124:127], v[140:143], v[144:147], v[124:127]\n"
        "ds_read_b64 v[224:225], %[ga] offset:192\n"
        "ds_read_b64 v[226:227], %[gb] offset:192\n"
        "v_mfma_f32_16x16x32_bf16 v[112:115], v[160:163], v[176:179], v[112:115]\n"
        "ds_read_b128 v[240:243], %[fa] offset:3776\n"
        "v_mfma_f32_16x16x32_bf16 v[116:119], v[160:163], v[180:183], v[116:119]\n"
        "ds_read_b128 v[244:247], %[fa] offset:3264\n"
        "v_mfma_f32_16x16x32_bf16 v[120:123], v[160:163], v[184:187], v[120:123]\n"
        "ds_read_b128 v[248:251], %[fa] offset:2752\n"
        "ds_read_b128 v[252:255], %[fa] offset:2240\n"
        "v_mfma_f32_16x16x32_bf16 v[124:127], v[160:163], v[188:191], v[124:127]\n"
        "ds_read_b64 v[228:229], %[ga] offset:704\n"
        "s_waitcnt lgkmcnt(15)\n"
        "v_mfma_f32_16x16x32_bf16 v[116:119], v[164:167], v[176:179], v[116:119]\n"
        "ds_read_b64 v[230:231], %[gb] offset:704\n"
        "v_mfma_f32_16x16x32_bf16 v[120:123], v[164:167], v[180:183], v[120:123]\n"
        "ds_read_b64 v[232:233], %[ga] offset:1216\n"
        "ds_read_b64 v[234:235], %[gb] offset:1216\n"
        "v_mfma_f32_16x16x32_bf16 v[124:127], v[164:167], v[184:187], v[124:127]\n"
        "ds_read_b64 v[236:237], %[ga] offset:1728\n"
        "v_mfma_f32_16x16x32_bf16 v[120:123], v[168:171], v[176:179], v[120:123]\n"
        "ds_read_b64 v[238:239], %[gb] offset:1728\n"
        "v_mfma_f32_16x16x32_bf16 v[124:127], v[168:171], v[180:183], v[124:127]\n"
        "v_mfma_f32_16x16x32_bf16 v[124:127], v[172:175], v[176:179], v[124:127]\n"
        "v_mfma_f32_16x16x32_bf16 v[112:115], v[192:195], v[208:211], v[112:115]\n"
        "ds_read_b64 v[128:129], %[ga] offset:256\n"
        "ds_read_b64 v[130:131], %[gb] offset:256\n"
        "v_mfma_f32_16x16x32_bf16 v[116:119], v[192:195], v[212:215], v[116:119]\n"
        "ds_read_b128 v[144:147], %[fa] offset:3840\n"
        "s_waitcnt lgkmcnt(15)\n"
        "v_mfma_f32_16x16x32_bf16 v[120:123], v[192:195], v[216:219], v[120:123]\n"
        "ds_read_b128 v[148:151], %[fa] offset:3328\n"
        "v_mfma_f32_16x16x32_bf16 v[124:127], v[192:195], v[220:223], v[124:127]\n"
        "ds_read_b128 v[152:155], %[fa] offset:2816\n"
        "ds_read_b128 v[156:159], %[fa] offset:2304\n"
        "v_mfma_f32_16x16x32_bf16 v[116:119], v[196:199], v[208:211], v[116:119]\n"
        "ds_read_b64 v[132:133], %[ga] offset:768\n"
        "v_mfma_f32_16x16x32_bf16 v[120:123], v[196:199], v[212:215], v[120:123]\n"
        "ds_read_b64 v[134:135], %[gb] offset:768\n"
        "v_mfma_f32_16x16x32_bf16 v[124:127], v[196:199], v[216:219], v[124:127]\n"
        "ds_read_b64 v[136:137], %[ga] offset:1280\n"
        "ds_read_b64 v[138:139], %[gb] offset:1280\n"
        "v_mfma_f32_16x16x32_bf16 v[120:123], v[200:203], v[208:211], v[120:123]\n"
        "ds_read_b64 v[140:141], %[ga] offset:1792\n"
        "v_mfma_f32_16x16x32_bf16 v[124:127], v[200:203], v[212:215], v[124:127]\n"
        "ds_read_b64 v[142:143], %[gb] offset:1792\n"
        "v_mfma_f32_16x16x32_bf16 v[124:127], v[204:207], v[208:211], v[124:127]\n"
        "ds_read_b64 v[160:161], %[ga] offset:320\n"
        "ds_read_b64 v[162:163], %[gb] offset:320\n"
        "s_waitcnt lgkmcnt(15)\n"
        "v_mfma_f32_16x16x32_bf16 v[112:115], v[224:227], v[240:243], v[112:115]\n"
        "ds_read_b128 v[176:179], %[fa] offset:3904\n"
        "v_mfma_f32_16x16x32_bf16 v[116:119], v[224:227], v[244:247], v[116:119]\n"
        "ds_read_b128 v[180:183], %[fa] offset:3392\n"
        "v_mfma_f32_16x16x32_bf16 v[120:123], v[224:227], v[248:251], v[120:123]\n"
        "ds_read_b128 v[184:187], %[fa] offset:2880\n"
        "ds_read_b128 v[188:191], %[fa] offset:2368\n"
        "v_mfma_f32_16x16x32_bf16 v[124:127], v[224:227], v[252:255], v[124:127]\n"
        "ds_read_b64 v[164:165], %[ga] offset:832\n"
        "v_mfma_f32_16x16x32_bf16 v[116:119], v[228:231], v[240:243], v[116:119]\n"
        "ds_read_b64 v[166:167], %[gb] offset:832\n"
        "v_mfma_f32_16x16x32_bf16 v[120:123], v[228:231], v[244:247], v[120:123]\n"
        "ds_read_b64 v[168:169], %[ga] offset:1344\n"
        "ds_read_b64 v[170:171], %[gb] offset:1344\n"
        "v_mfma_f32_16x16x32_bf16 v[124:127], v[228:231], v[248:251], v[124:127]\n"
        "ds_read_b64 v[172:173], %[ga] offset:1856\n"
        "v_mfma_f32_16x16x32_bf16 v[120:123], v[232:235], v[240:243], v[120:123]\n"
        "ds_read_b64 v[174:175], %[gb] offset:1856\n"
        "v_mfma_f32_16x16x32_bf16 v[124:127], v[232:235], v[244:247], v[124:127]\n"
        "s_waitcnt lgkmcnt(15)\n"
        "v_mfma_f32_16x16x32_bf16 v[124:127], v[236:239], v[240:243], v[124:127]\n"
        "v_mfma_f32_16x16x32_bf16 v[112:115], v[128:131], v[144:147], v[112:115]\n"
        "ds_read_b64 v[192:193], %[ga] offset:384\n"
        "ds_read_b64 v[194:195], %[gb] offset:384\n"
        "v_mfma_f32_16x16x32_bf16 v[116:119], v[128:131], v[148:151], v[116:119]\n"
        "ds_read_b128 v[208:211], %[fa] offset:3968\n"
        "v_mfma_f32_16x16x32_bf16 v[120:123], v[128:131], v[152:155], v[120:123]\n"
        "ds_read_b128 v[212:215], %[fa] offset:3456\n"
        "v_mfma_f32_16x16x32_bf16 v[124:127], v[128:131], v[156:159], v[124:127]\n"
        "ds_read_b128 v[216:219], %[fa] offset:2944\n"
        "ds_read_b128 v[220:223], %[fa] offset:2432\n"
        "v_mfma_f32_16x16x32_bf16 v[116:119], v[132:135], v[144:147], v[116:119]\n"
        "ds_read_b64 v[196:197], %[ga] offset:896\n"
        "v_mfma_f32_16x16x32_bf16 v[120:123], v[132:135], v[148:151], v[120:123]\n"
        "ds_read_b64 v[198:199], %[gb] offset:896\n"
        "v_mfma_f32_16x16x32_bf16 v[124:127], v[132:135], v[152:155], v[124:127]\n"
        "ds_read_b64 v[200:201], %[ga] offset:1408\n"
        "ds_read_b64 v[202:203], %[gb] offset:1408\n"
        "s_waitcnt lgkmcnt(15)\n"
        "v_mfma_f32_16x16x32_bf16 v[120:123], v[136:139], v[144:147], v[120:123]\n"
        "ds_read_b64 v[204:205], %[ga] offset:1920\n"
        "v_mfma_f32_16x16x32_bf16 v[124:127], v[136:139], v[148:151], v[124:127]\n"
        "ds_read_b64 v[206:207], %[gb] offset:1920\n"
        "v_mfma_f32_16x16x32_bf16 v[124:127], v[140:143], v[144:147], v[124:127]\n"
        "ds_read_b64 v[224:225], %[ga] offset:448\n"
        "ds_read_b64 v[226:227], %[gb] offset:448\n"
        "v_mfma_f32_16x16x32_bf16 v[112:115], v[160:163], v[176:179], v[112:115]\n"
        "ds_read_b128 v[240:243], %[fa] offset:4032\n"
        "v_mfma_f32_16x16x32_bf16 v[116:119], v[160:163], v[180:183], v[116:119]\n"
        "ds_read_b128 v[244:247], %[fa] offset:3520\n"
        "v_mfma_f32_16x16x32_bf16 v[120:123], v[160:163], v[184:187], v[120:123]\n"
        "ds_read_b128 v[248:251], %[fa] offset:3008\n"
        "ds_read_b128 v[252:255], %[fa] offset:2496\n"
        "v_mfma_f32_16x16x32_bf16 v[124:127], v[160:163], v[188:191], v[124:127]\n"
        "ds_read_b64 v[228:229], %[ga] offset:960\n"
        "s_waitcnt lgkmcnt(15)\n"
        "v_mfma_f32_16x16x32_bf16 v[116:119], v[164:167], v[176:179], v[116:119]\n"
        "ds_read_b64 v[230:231], %[gb] offset:960\n"
        "v_mfma_f32_16x16x32_bf16 v[120:123], v[164:167], v[180:183], v[120:123]\n"
        "ds_read_b64 v[232:233], %[ga] offset:1472\n"
        "ds_read_b64 v[234:235], %[gb] offset:1472\n"
        "v_mfma_f32_16x16x32_bf16 v[124:127], v[164:167], v[184:187], v[124:127]\n"
        "ds_read_b64 v[236:237], %[ga] offset:1984\n"
        "v_mfma_f32_16x16x32_bf16 v[120:123], v[168:171], v[176:179], v[120:123]\n"
        "ds_read_b64 v[238:239], %[gb] offset:1984\n"
        "v_mfma_f32_16x16x32_bf16 v[124:127], v[168:171], v[180:183], v[124:127]\n"
        "v_mfma_f32_16x16x32_bf16 v[124:127], v[172:175], v[176:179], v[124:127]\n"
        "v_mfma_f32_16x16x32_bf16 v[112:115], v[192:195], v[208:211], v[112:115]\n"
        "ds_read_b64 v[128:129], %[ga] offset:0\n"
        "ds_read_b64 v[130:131], %[gb] offset:0\n"
        "v_mfma_f32_16x16x32_bf16 v[116:119], v[192:195], v[212:215], v[116:119]\n"
        "ds_read_b128 v[176:179], %[fa] offset:1536\n"
        "s_waitcnt lgkmcnt(15)\n"
        "v_mfma_f32_16x16x32_bf16 v[120:123], v[192:195], v[216:219], v[120:123]\n"
        "ds_read_b128 v[180:183], %[fa] offset:1024\n"
        "v_mfma_f32_16x16x32_bf16 v[124:127], v[192:195], v[220:223], v[124:127]\n"
        "ds_read_b128 v[184:187], %[fa] offset:512\n"
        "ds_read_b128 v[188:191], %[fa] offset:0\n"
        "v_mfma_f32_16x16x32_bf16 v[116:119], v[196:199], v[208:211], v[116:119]\n"
        "ds_read_b64 v[132:133], %[ga] offset:512\n"
        "v_mfma_f32_16x16x32_bf16 v[120:123], v[196:199], v[212:215], v[120:123]\n"
        "ds_read_b64 v[134:135], %[gb] offset:512\n"
        "v_mfma_f32_16x16x32_bf16 v[124:127], v[196:199], v[216:219], v[124:127]\n"
        "ds_read_b128 v[172:175], %[fa] offset:2048\n"
        "ds_read_b64 v[136:137], %[ga] offset:1024\n"
        "v_mfma_f32_16x16x32_bf16 v[120:123], v[200:203], v[208:211], v[120:123]\n"
        "ds_read_b64 v[138:139], %[gb] offset:1024\n"
        "v_mfma_f32_16x16x32_bf16 v[124:127], v[200:203], v[212:215], v[124:127]\n"
        "ds_read_b128 v[168:171], %[fa] offset:2560\n"
        "v_mfma_f32_16x16x32_bf16 v[124:127], v[204:207], v[208:211], v[124:127]\n"
        "ds_read_b64 v[140:141], %[ga] offset:1536\n"
        "ds_read_b64 v[142:143], %[gb] offset:1536\n"
        "s_waitcnt lgkmcnt(15)\n"
        "v_mfma_f32_16x16x32_bf16 v[112:115], v[224:227], v[240:243], v[112:115]\n"
        "ds_read_b128 v[164:167], %[fa] offset:3072\n"
        "v_mfma_f32_16x16x32_bf16 v[116:119], v[224:227], v[244:247], v[116:119]\n"
        "ds_read_b64 v[144:145], %[ga] offset:2048\n"
        "v_mfma_f32_16x16x32_bf16 v[120:123], v[224:227], v[248:251], v[120:123]\n"
        "ds_read_b64 v[146:147], %[gb] offset:2048\n"
        "ds_read_b128 v[160:163], %[fa] offset:3584\n"
        "v_mfma_f32_16x16x32_bf16 v[124:127], v[224:227], v[252:255], v[124:127]\n"
        "ds_read_b64 v[148:149], %[ga] offset:2560\n"
        "v_mfma_f32_16x16x32_bf16 v[116:119], v[228:231], v[240:243], v[116:119]\n"
        "ds_read_b64 v[150:151], %[gb] offset:2560\n"
        "v_mfma_f32_16x16x32_bf16 v[120:123], v[228:231], v[244:247], v[120:123]\n"
        "ds_read_b64 v[152:153], %[ga] offset:3072\n"
        "ds_read_b64 v[154:155], %[gb] offset:3072\n"
        "v_mfma_f32_16x16x32_bf16 v[124:127], v[228:231], v[248:251], v[124:127]\n"
        "ds_read_b64 v[156:157], %[ga] offset:3584\n"
        "v_mfma_f32_16x16x32_bf16 v[120:123], v[232:235], v[240:243], v[120:123]\n"
        "ds_read_b64 v[158:159], %[gb] offset:3584\n"
        "v_mfma_f32_16x16x32_bf16 v[124:127], v[232:235], v[244:247], v[124:127]\n"
        "s_waitcnt lgkmcnt(15)\n"
        "v_mfma_f32_16x16x32_bf16 v[124:127], v[236:239], v[240:243], v[124:127]\n"
        "v_mfma_f32_16x16x32_bf16 %[a4], v[128:131], v[176:179], 0\n"
        "ds_read_b64 v[192:193], %[ga] offset:64\n"
        "v_mfma_f32_16x16x32_bf16 %[a5], v[128:131], v[180:183], 0\n"
        "ds_read_b64 v[194:195], %[gb] offset:64\n"
        "v_mfma_f32_16x16x32_bf16 %[a6], v[128:131], v[184:187], 0\n"
        "ds_read_b128 v[240:243], %[fa] offset:1600\n"
        "v_mfma_f32_16x16x32_bf16 %[a7], v[128:131], v[188:191], 0\n"
        "ds_read_b128 v[244:247], %[fa] offset:1088\n"
        "v_mfma_f32_16x16x32_bf16 %[a7], v[132:135], v[184:187], %[a7]\n"
        "ds_read_b128 v[248:251], %[fa] offset:576\n"
        "v_mfma_f32_16x16x32_bf16 %[a6], v[132:135], v[180:183], %[a6]\n"
        "ds_read_b128 v[252:255], %[fa] offset:64\n"
        "v_mfma_f32_16x16x32_bf16 %[a5], v[132:135], v[176:179], %[a5]\n"
        "ds_read_b64 v[196:197], %[ga] offset:576\n"
        "v_mfma_f32_16x16x32_bf16 %[a4], v[132:135], v[172:175], %[a4]\n"
        "ds_read_b64 v[198:199], %[gb] offset:576\n"
        "s_waitcnt lgkmcnt(15)\n"
        "v_mfma_f32_16x16x32_bf16 %[a7], v[136:139], v[180:183], %[a7]\n"
        "ds_read_b128 v[236:239], %[fa] offset:2112\n"
        "v_cvt_pk_bf16_f32 v104, v112, v113\n"
        "v_cvt_pk_bf16_f32 v105, v114, v115\n"
        "v_mfma_f32_16x16x32_bf16 %[a6], v[136:139], v[176:179], %[a6]\n"
        "ds_read_b64 v[200:201], %[ga] offset:1088\n"
        "ds_write_b64 %[ya], v[104:105] offset:0\n"
        "v_mfma_f32_16x16x32_bf16 %[a5], v[136:139], v[172:175], %[a5]\n"
        "ds_read_b64 v[202:203], %[gb] offset:1088\n"
        "v_cvt_pk_bf16_f32 v106, v116, v117\n"
        "v_cvt_pk_bf16_f32 v107, v118, v119\n"
        "v_mfma_f32_16x16x32_bf16 %[a4], v[136:139], v[168:171], %[a4]\n"
        "ds_read_b128 v[232:235], %[fa] offset:2624\n"
        "ds_write_b64 %[ya], v[106:107] offset:512\n"
        "v_mfma_f32_16x16x32_bf16 %[a7], v[140:143], v[176:179], %[a7]\n"
        "ds_read_b64 v[204:205], %[ga] offset:1600\n"
        "v_cvt_pk_bf16_f32 v108, v120, v121\n"
        "v_cvt_pk_bf16_f32 v109, v122, v123\n"
        "v_mfma_f32_16x16x32_bf16 %[a6], v[140:143], v[172:175], %[a6]\n"
        "ds_read_b64 v[206:207], %[gb] offset:1600\n"
        "ds_write_b64 %[ya], v[108:109] offset:1024\n"
        "v_mfma_f32_16x16x32_bf16 %[a5], v[140:143], v[168:171], %[a5]\n"
        "ds_read_b128 v[228:231], %[fa] offset:3136\n"
        "v_cvt_pk_bf16_f32 v110, v124, v125\n"
        "v_cvt_pk_bf16_f32 v111, v126, v127\n"
        "v_mfma_f32_16x16x32_bf16 %[a4], v[140:143], v[164:167], %[a4]\n"
        "ds_read_b64 v[208:209], %[ga] offset:2112\n"
        "ds_write_b64 %[ya], v[110:111] offset:1536\n"
        "v_mfma_f32_16x16x32_bf16 %[a7], v[144:147], v[172:175], %[a7]\n"
        "ds_read_b64 v[210:211], %[gb] offset:2112\n"
        "v_mfma_f32_16x16x32_bf16 %[a6], v[144:147], v[168:171], %[a6]\n"
        "ds_read_b128 v[224:227], %[fa] offset:3648\n"
        "s_mov_b64 exec, 1\n"
        "ds_write_b32 %[yf], %[one]\n"
        "s_mov_b64 exec, -1\n"
        "v_mfma_f32_16x16x32_bf16 %[a5], v[144:147], v[164:167], %[a5]\n"
        "ds_read_b64 v[212:213], %[ga] offset:2624\n"
        "s_waitcnt lgkmcnt(15)\n"
        "v_mfma_f32_16x16x32_bf16 %[a4], v[144:147], v[160:163], %[a4]\n"
        "ds_read_b64 v[214:215], %[gb] offset:2624\n"
        "v_mfma_f32_16x16x32_bf16 %[a5], v[148:151], v[160:163], %[a5]\n"
        "ds_read_b64 v[216:217], %[ga] offset:3136\n"
        "v_mfma_f32_16x16x32_bf16 %[a6], v[148:151], v[164:167], %[a6]\n"
        "ds_read_b64 v[218:219], %[gb] offset:3136\n"
        "v_mfma_f32_16x16x32_bf16 %[a7], v[148:151], v[168:171], %[a7]\n"
        "ds_read_b64 v[220:221], %[ga] offset:3648\n"
        "v_mfma_f32_16x16x32_bf16 %[a6], v[152:155], v[160:163], %[a6]\n"
        "ds_read_b64 v[222:223], %[gb] offset:3648\n"
        "v_mfma_f32_16x16x32_bf16 %[a7], v[152:155], v[164:167], %[a7]\n"
        "v_mfma_f32_16x16x32_bf16 %[a7], v[156:159], v[160:163], %[a7]\n"
        "s_waitcnt lgkmcnt(8)\n"
        "s_mov_b32 %[cnt], 3\n"
        "1:\n"
        "v_mfma_f32_16x16x32_bf16 %[a4], v[192:195], v[240:243], %[a4]\n"
        "ds_read_b64 v[128:129], %[ga] offset:128\n"
        "v_mfma_f32_16x16x32_bf16 %[a5], v[192:195], v[244:247], %[a5]\n"
        "ds_read_b64 v[130:131], %[gb] offset:128\n"
        "v_mfma_f32_16x16x32_bf16 %[a6], v[192:195], v[248:251], %[a6]\n"
        "ds_read_b128 v[176:179], %[fa] offset:1664\n"
        "v_mfma_f32_16x16x32_bf16 %[a7], v[192:195], v[252:255], %[a7]\n"
        "ds_read_b128 v[180:183], %[fa] offset:1152\n"
        "v_mfma_f32_16x16x32_bf16 %[a7], v[196:199], v[248:251], %[a7]\n"
        "ds_read_b128 v[184:187], %[fa] offset:640\n"
        "v_mfma_f32_16x16x32_bf16 %[a6], v[196:199], v[244:247], %[a6]\n"
        "ds_read_b128 v[188:191], %[fa] offset:128\n"
        "v_mfma_f32_16x16x32_bf16 %[a5], v[196:199], v[240:243], %[a5]\n"
        "ds_read_b64 v[132:133], %[ga] offset:640\n"
        "v_mfma_f32_16x16x32_bf16 %[a4], v[196:199], v[236:239], %[a4]\n"
        "ds_read_b64 v[134:135], %[gb] offset:640\n"
        "v_mfma_f32_16x16x32_bf16 %[a7], v[200:203], v[244:247], %[a7]\n"
        "ds_read_b128 v[172:175], %[fa] offset:2176\n"
        "v_mfma_f32_16x16x32_bf16 %[a6], v[200:203], v[240:243], %[a6]\n"
        "ds_read_b64 v[136:137], %[ga] offset:1152\n"
        "v_mfma_f32_16x16x32_bf16 %[a5], v[200:203], v[236:239], %[a5]\n"
        "ds_read_b64 v[138:139], %[gb] offset:1152\n"
        "v_mfma_f32_16x16x32_bf16 %[a4], v[200:203], v[232:235], %[a4]\n"
        "ds_read_b128 v[168:171], %[fa] offset:2688\n"
        "v_mfma_f32_16x16x32_bf16 %[a7], v[204:207], v[240:243], %[a7]\n"
        "ds_read_b64 v[140:141], %[ga] offset:1664\n"
        "v_mfma_f32_16x16x32_bf16 %[a6], v[204:207], v[236:239], %[a6]\n"
        "ds_read_b64 v[142:143], %[gb] offset:1664\n"
        "v_mfma_f32_16x16x32_bf16 %[a5], v[204:207], v[232:235], %[a5]\n"
        "ds_read_b128 v[164:167], %[fa] offset:3200\n"
        "v_mfma_f32_16x16x32_bf16 %[a4], v[204:207], v[228:231], %[a4]\n"
        "ds_read_b64 v[144:145], %[ga] offset:2176\n"
        "v_mfma_f32_16x16x32_bf16 %[a7], v[208:211], v[236:239], %[a7]\n"
        "ds_read_b64 v[146:147], %[gb] offset:2176\n"
        "v_mfma_f32_16x16x32_bf16 %[a6], v[208:211], v[232:235], %[a6]\n"
        "ds_read_b128 v[160:163], %[fa] offset:3712\n"
        "v_mfma_f32_16x16x32_bf16 %[a5], v[208:211], v[228:231], %[a5]\n"
        "ds_read_b64 v[148:149], %[ga] offset:2688\n"
        "s_waitcnt lgkmcnt(15)\n"
        "v_mfma_f32_16x16x32_bf16 %[a4], v[208:211], v[224:227], %[a4]\n"
        "ds_read_b64 v[150:151], %[gb] offset:2688\n"
        "v_mfma_f32_16x16x32_bf16 %[a5], v[212:215], v[224:227], %[a5]\n"
        "ds_read_b64 v[152:153], %[ga] offset:3200\n"
        "v_mfma_f32_16x16x32_bf16 %[a6], v[212:215], v[228:231], %[a6]\n"
        "ds_read_b64 v[154:155], %[gb] offset:3200\n"
        "v_mfma_f32_16x16x32_bf16 %[a7], v[212:215], v[232:235], %[a7]\n"
        "ds_read_b64 v[156:157], %[ga] offset:3712\n"
        "v_mfma_f32_16x16x32_bf16 %[a6], v[216:219], v[224:227], %[a6]\n"
        "ds_read_b64 v[158:159], %[gb] offset:3712\n"
        "v_mfma_f32_16x16x32_bf16 %[a7], v[216:219], v[228:231], %[a7]\n"
        "v_mfma_f32_16x16x32_bf16 %[a7], v[220:223], v[224:227], %[a7]\n"
        "v_mfma_f32_16x16x32_bf16 %[a4], v[128:131], v[176:179], %[a4]\n"
        "ds_read_b64 v[192:193], %[ga] offset:192\n"
        "v_mfma_f32_16x16x32_bf16 %[a5], v[128:131], v[180:183], %[a5]\n"
        "ds_read_b64 v[194:195], %[gb] offset:192\n"
        "s_waitcnt lgkmcnt(15)\n"
        "v_mfma_f32_16x16x32_bf16 %[a6], v[128:131], v[184:187], %[a6]\n"
        "ds_read_b128 v[240:243], %[fa] offset:1728\n"
        "v_mfma_f32_16x16x32_bf16 %[a7], v[128:131], v[188:191], %[a7]\n"
        "ds_read_b128 v[244:247], %[fa] offset:1216\n"
        "v_mfma_f32_16x16x32_bf16 %[a7], v[132:135], v[184:187], %[a7]\n"
        "ds_read_b128 v[248:251], %[fa] offset:704\n"
        "v_mfma_f32_16x16x32_bf16 %[a6], v[132:135], v[180:183], %[a6]\n"
        "ds_read_b128 v[252:255], %[fa] offset:192\n"
        "v_mfma_f32_16x16x32_bf16 %[a5], v[132:135], v[176:179], %[a5]\n"
        "ds_read_b64 v[196:197], %[ga] offset:704\n"
        "v_mfma_f32_16x16x32_bf16 %[a4], v[132:135], v[172:175], %[a4]\n"
        "ds_read_b64 v[198:199], %[gb] offset:704\n"
        "v_mfma_f32_16x16x32_bf16 %[a7], v[136:139], v[180:183], %[a7]\n"
        "ds_read_b128 v[236:239], %[fa] offset:2240\n"
        "v_mfma_f32_16x16x32_bf16 %[a6], v[136:139], v[176:179], %[a6]\n"
        "ds_read_b64 v[200:201], %[ga] offset:1216\n"
        "v_mfma_f32_16x16x32_bf16 %[a5], v[136:139], v[172:175], %[a5]\n"
        "ds_read_b64 v[202:203], %[gb] offset:1216\n"
        "s_waitcnt lgkmcnt(15)\n"
        "v_mfma_f32_16x16x32_bf16 %[a4], v[136:139], v[168:171], %[a4]\n"
        "ds_read_b128 v[232:235], %[fa] offset:2752\n"
        "v_mfma_f32_16x16x32_bf16 %[a7], v[140:143], v[176:179], %[a7]\n"
        "ds_read_b64 v[204:205], %[ga] offset:1728\n"
        "v_mfma_f32_16x16x32_bf16 %[a6], v[140:143], v[172:175], %[a6]\n"
        "ds_read_b64 v[206:207], %[gb] offset:1728\n"
        "v_mfma_f32_16x16x32_bf16 %[a5], v[140:143], v[168:171], %[a5]\n"
        "ds_read_b128 v[228:231], %[fa] offset:3264\n"
        "v_mfma_f32_16x16x32_bf16 %[a4], v[140:143], v[164:167], %[a4]\n"
        "ds_read_b64 v[208:209], %[ga] offset:2240\n"
        "v_mfma_f32_16x16x32_bf16 %[a7], v[144:147], v[172:175], %[a7]\n"
        "ds_read_b64 v[210:211], %[gb] offset:2240\n"
        "v_mfma_f32_16x16x32_bf16 %[a6], v[144:147], v[168:171], %[a6]\n"
        "ds_read_b128 v[224:227], %[fa] offset:3776\n"
        "v_mfma_f32_16x16x32_bf16 %[a5], v[144:147], v[164:167], %[a5]\n"
        "ds_read_b64 v[212:213], %[ga] offset:2752\n"
        "v_mfma_f32_16x16x32_bf16 %[a4], v[144:147], v[160:163], %[a4]\n"
        "ds_read_b64 v[214:215], %[gb] offset:2752\n"
        "v_mfma_f32_16x16x32_bf16 %[a5], v[148:151], v[160:163], %[a5]\n"
        "ds_read_b64 v[216:217], %[ga] offset:3264\n"
        "v_mfma_f32_16x16x32_bf16 %[a6], v[148:151], v[164:167], %[a6]\n"
        "ds_read_b64 v[218:219], %[gb] offset:3264\n"
        "v_mfma_f32_16x16x32_bf16 %[a7], v[148:151], v[168:171], %[a7]\n"
        "ds_read_b64 v[220:221], %[ga] offset:3776\n"
        "s_waitcnt lgkmcnt(15)\n"
        "v_mfma_f32_16x16x32_bf16 %[a6], v[152:155], v[160:163], %[a6]\n"
        "ds_read_b64 v[222:223], %[gb] offset:3776\n"
        "v_mfma_f32_16x16x32_bf16 %[a7], v[152:155], v[164:167], %[a7]\n"
        "v_mfma_f32_16x16x32_bf16 %[a7], v[156:159], v[160:163], %[a7]\n"
        "v_add_u32 %[ga], 128, %[ga]\n"
        "v_add_u32 %[gb], 128, %[gb]\n"
        "v_add_u32 %[fa], 128, %[fa]\n"
        "s_waitcnt lgkmcnt(8)\n"
        "s_sub_u32 %[cnt], %[cnt], 1\n"
        "s_cmp_lg_u32 %[cnt], 0\n"
        "s_cbranch_scc1 1b\n"
        "v_mfma_f32_16x16x32_bf16 %[a4], v[192:195], v[240:243], %[a4]\n"
        "v_mfma_f32_16x16x32_bf16 %[a5], v[192:195], v[244:247], %[a5]\n"
        "v_mfma_f32_16x16x32_bf16 %[a6], v[192:195], v[248:251], %[a6]\n"
        "v_mfma_f32_16x16x32_bf16 %[a7], v[192:195], v[252:255], %[a7]\n"
        "v_mfma_f32_16x16x32_bf16 %[a7], v[196:199], v[248:251], %[a7]\n"
        "v_mfma_f32_16x16x32_bf16 %[a6], v[196:199], v[244:247], %[a6]\n"
        "v_mfma_f32_16x16x32_bf16 %[a5], v[196:199], v[240:243], %[a5]\n"
        "v_mfma_f32_16x16x32_bf16 %[a4], v[196:199], v[236:239], %[a4]\n"
        "v_mfma_f32_16x16x32_bf16 %[a7], v[200:203], v[244:247], %[a7]\n"
        "v_mfma_f32_16x16x32_bf16 %[a6], v[200:203], v[240:243], %[a6]\n"
        "v_mfma_f32_16x16x32_bf16 %[a5], v[200:203], v[236:239], %[a5]\n"
        "v_mfma_f32_16x16x32_bf16 %[a4], v[200:203], v[232:235], %[a4]\n"
        "v_mfma_f32_16x16x32_bf16 %[a7], v[204:207], v[240:243], %[a7]\n"
        "v_mfma_f32_16x16x32_bf16 %[a6], v[204:207], v[236:239], %[a6]\n"
        "v_mfma_f32_16x16x32_bf16 %[a5], v[204:207], v[232:235], %[a5]\n"
        "v_mfma_f32_16x16x32_bf16 %[a4], v[204:207], v[228:231], %[a4]\n"
        "s_waitcnt lgkmcnt(7)\n"
        "v_mfma_f32_16x16x32_bf16 %[a7], v[208:211], v[236:239], %[a7]\n"
        "v_mfma_f32_16x16x32_bf16 %[a6], v[208:211], v[232:235], %[a6]\n"
        "v_mfma_f32_16x16x32_bf16 %[a5], v[208:211], v[228:231], %[a5]\n"
        "s_waitcnt lgkmcnt(6)\n"
        "v_mfma_f32_16x16x32_bf16 %[a4], v[208:211], v[224:227], %[a4]\n"
        "s_waitcnt lgkmcnt(4)\n"
        "v_mfma_f32_16x16x32_bf16 %[a5], v[212:215], v[224:227], %[a5]\n"
        "v_mfma_f32_16x16x32_bf16 %[a6], v[212:215], v[228:231], %[a6]\n"
        "v_mfma_f32_16x16x32_bf16 %[a7], v[212:215], v[232:235], %[a7]\n"
        "s_waitcnt lgkmcnt(2)\n"
        "v_mfma_f32_16x16x32_bf16 %[a6], v[216:219], v[224:227], %[a6]\n"
        "v_mfma_f32_16x16x32_bf16 %[a7], v[216:219], v[228:231], %[a7]\n"
        "s_waitcnt lgkmcnt(0)\n"
        "v_mfma_f32_16x16x32_bf16 %[a7], v[220:223], v[224:227], %[a7]\n"
        "s_setprio 0\n"
        "s_nop 7\n"
        "s_nop 1\n"
        : [ga] "+v"(ga), [gb] "+v"(gb), [fa] "+v"(fa), [a4] "=&v"(a4), [a5] "=&v"(a5), [a6] "=&v"(a6), [a7] "=&v"(a7), [cnt] "=&s"(cnt)
        : [ya] "v"(ya), [yf] "v"(yf), [one] "v"(1)
        : "memory", "scc", "v104", "v105", "v106", "v107", "v108", "v109", "v110", "v111", "v112", "v113", "v114", "v115", "v116", "v117", "v118", "v119", "v120", "v121", "v122", "v123", "v124", "v125", "v126", "v127", "v128", "v129", "v130", "v131", "v132", "v133", "v134", "v135", "v136", "v137", "v138", "v139", "v140", "v141", "v142", "v143", "v144", "v145", "v146", "v147", "v148", "v149", "v150", "v151", "v152", "v153", "v154", "v155", "v156", "v157", "v158", "v159", "v160", "v161", "v162", "v163", "v164", "v165", "v166", "v167", "v168", "v169", "v170", "v171", "v172", "v173", "v174", "v175", "v176", "v177", "v178", "v179", "v180", "v181", "v182", "v183", "v184", "v185", "v186", "v187", "v188", "v189", "v190", "v191", "v192", "v193", "v194", "v195", "v196", "v197", "v198", "v199", "v200", "v201", "v202", "v203", "v204", "v205", "v206", "v207", "v208", "v209", "v210", "v211", "v212", "v213", "v214", "v215", "v216", "v217", "v218", "v219", "v220", "v221", "v222", "v223", "v224", "v225", "v226", "v227", "v228", "v229", "v230", "v231", "v232", "v233", "v234", "v235", "v236", "v237", "v238", "v239", "v240", "v241", "v242", "v243", "v244", "v245", "v246", "v247", "v248", "v249", "v250", "v251", "v252", "v253", "v254", "v255");
}

__device__ __forceinline__ float dot2(u32 a, u32 b, float c) {
    return __builtin_amdgcn_fdot2_f32_bf16(__builtin_bit_cast(bf16x2, a), __builtin_bit_cast(bf16x2, b), c, false);
}
__device__ __forceinline__ void lds_wait4(int addr, u32 want) {
#pragma unroll 1
    for (u32 spins = 0; spins < (1u << 22); ++spins) {
        const u32x4 f = *(const volatile LAS u32x4*)(size_t)(u32)addr;
        if (__builtin_amdgcn_readfirstlane(f[0] & f[1] & f[2] & f[3]) >= want) break;
        __builtin_amdgcn_s_sleep(3);
    }
    asm volatile("" ::: "memory");
}

constexpr int C_T = L_MISC, C_PUB = L_MISC + 4, C_TB = L_MISC + 8, C_KB = L_MISC + 12, C_KT = L_MISC + 16, C_GO = L_MISC + 20, C_GO2 = L_MISC + 24, C_Y = L_MISC + 32;

__global__ void __launch_bounds__(NT, 2) fused_kernel(Params P) {
    extern __shared__ __attribute__((aligned(16))) char smem_[];
    const int tid = threadIdx.x, lane = tid & 63;
    const int wv = __builtin_amdgcn_readfirstlane(tid >> 6);
    const int w = blockIdx.x, grp = w & 7, ti = w >> 3;
    const int d0 = 64 * grp + 2 * ti;
    char* ws = P.ws;
    STAMP_DECL
    stamp(ws, 0);
    u32 pfv;
    {
        u64 pc; u32 len;
        asm volatile("s_getpc_b64 %0\n.Lfk_pf_start:\n\ts_mov_b32 %1, .Lfk_pf_end-.Lfk_pf_start" : "=s"(pc), "=s"(len));
        u32 off = (u32)tid * 64u;
        off = off < len - 4u ? off : len - 4u;
        pfv = *(const GAS u32*)(pc + (off & ~3u));
    }
    const __amdgpu_buffer_rsrc_t sig_rsrc = __builtin_amdgcn_make_buffer_rsrc(ws + WS_SIG, 0, 2 * D * S * 2, 0x00020000);
    const __amdgpu_buffer_rsrc_t g_rsrc = __builtin_amdgcn_make_buffer_rsrc(ws + WS_G, 0, S * 4, 0x00020000);

    u32x4 lk[2], lv[2], lq[2], gq[2], lh, gwv, sm[2], gt0;
    u32x2 skw[2], sm2[2], sme, gt1, xt0, xt1;
    u32 pl, pa, pb, pc, pd, pe, pf;
    u64 tag;
    const bool has_halo = (wv == 0) && (ti > 0);
    const int kw = wv - 4, kt = tid - 256, kch = kw >> 1;
    if (wv < 4) {
        const size_t toff = (size_t)(64 * ti + 16 * wv + (lane >> 3)) * D + 64 * grp + 8 * (lane & 7);
#pragma unroll
        for (int u = 0; u < 2; ++u) { lk[u] = *(const u32x4*)(P.k + toff + 8 * u * D); lv[u] = *(const u32x4*)(P.v + toff + 8 * u * D); lq[u] = *(const u32x4*)(P.q + toff + 8 * u * D); }
        lh = *(const u32x4*)(P.k + (has_halo ? toff - 8 * D : toff));
#pragma unroll
        for (int u = 0; u < 2; ++u) {
            skw[u] = *(const u32x2*)(P.sk + (64 * grp + 16 * wv + 8 * u + (lane >> 3)) * 4);
            gq[u] = *(const u32x4*)(P.q + (size_t)(8 * w + 2 * wv + u) * D + 8 * lane);
        }
        gwv = *(const u32x4*)(P.gw + 8 * lane);
        tag = *(const GAS u64*)(ws + WS_EPOCH);
        if (tid < 3) lds_st<u32x4>(L_MISC + 16 * tid, (u32x4){0, 0, 0, 0});
        if (tid >= 64 && tid < 64 + 4 * 30) lds_st<u32x4>(L_SIG + ((tid - 64) / 30) * SIG_ST + 4096 + ((tid - 64) % 30) * 16, (u32x4){0, 0, 0, 0});
    } else {
        const int pidx = (d0 + kch) * NS + 64 * (kw & 1) + lane;
        pl = P.ldt[d0 + kch];
        pa = P.are[pidx]; pb = P.aim[pidx]; pc = P.bre[pidx]; pd = P.bim[pidx]; pe = P.cre[pidx]; pf = P.cim[pidx];
#pragma unroll
        for (int ch = 0; ch < 2; ++ch) {
            const u16* ssrc = P.ssm + (size_t)(d0 + ch) * S + 8 * kt;
            sm[ch] = *(const u32x4*)ssrc;
            sm2[ch] = *(const u32x2*)(ssrc + (kt < 255 ? 8 : 0));
        }
        sme = *(const u32x2*)(P.ssm + (size_t)(d0 + ((lane >> 2) & 1)) * S);
        gt0 = *(const u32x4*)&idx_tabs.g[kt][0];
        gt1 = *(const u32x2*)&idx_tabs.g[kt][8];
        xt0 = *(const u32x2*)&idx_tabs.x[kw % 3][lane][0]; xt1 = *(const u32x2*)&idx_tabs.x[(kw + 1) % 3][lane][0];
        tag = *(const GAS u64*)(ws + WS_EPOCH);
    }
    __builtin_amdgcn_sched_barrier(0);
    __syncthreads();
    __builtin_amdgcn_sched_barrier(0);

    if (wv < 4) {
        __builtin_amdgcn_s_setprio(2);
        {
            const int r = lane >> 3, o = lane & 7;
            const u32 sel = (r & 1) ? 0x07060302u : 0x01000504u;
            const int c = 8 * o + r;
#pragma unroll
            for (int u = 0; u < 2; ++u) {
                const int a = L_T + c * 128 + (((7 - 2 * wv - u) ^ o) << 4);
                u32x4 t3[3] = {lk[u], lv[u], lq[u]};
                tr8n<3>(t3, sel);
                lds_st<u32x4>(a + 8192, t3[0]);
                lds_st<u32x4>(a + 16384, t3[1]);
                lds_st<u32x4>(a, t3[2]);
            }
            if (wv == 0) { u32x4 h1[1] = {lh}; tr8n<1>(h1, sel); if (!has_halo) h1[0] = (u32x4){0, 0, 0, 0}; lds_st<u32x4>(L_H + c * 16, h1[0]); }
        }
        lds_arrive(C_T, lane);
        stamp(ws, 1);
        lds_await(C_T, 4);
#pragma unroll
        for (int u = 0; u < 2; ++u) {
            const int v8 = 2 * wv + u, oct = lane & 7, c = 8 * v8 + (lane >> 3);
            const int a = L_T + c * 128 + ((oct ^ v8) << 4);
            const u32x4 tk = lds_ld<u32x4>(a + 8192);
            const int an = oct < 7 ? (L_T + 8192 + c * 128 + (((oct + 1) ^ v8) << 4)) : (L_H + c * 16);
            const u32x2 tn = lds_ld<u32x2>(an);
            const u32x4 tv = lds_ld<u32x4>(a + 16384), tq = lds_ld<u32x4>(a);
            const u32 go = ((64 * grp + c) * S + 1984 - 64 * ti + 8 * oct) * 2;
            __builtin_amdgcn_raw_buffer_store_b128(tq, sig_rsrc, go + D * S * 2, 0, 16);
            const u32 dd[6] = {tk[0], tk[1], tk[2], tk[3], tn[0], tn[1]};
            float kv[8];
#pragma unroll
            for (int m = 0; m < 4; ++m) {
                const u32 o0 = __builtin_amdgcn_alignbit(dd[m + 1], dd[m], 16), o1 = __builtin_amdgcn_alignbit(dd[m + 2], dd[m + 1], 16);
                kv[2 * m] = dot2(dd[m], skw[u][0], dot2(dd[m + 1], skw[u][1], 0.f)) * lo2f(tv[m]);
                kv[2 * m + 1] = dot2(o0, skw[u][0], dot2(o1, skw[u][1], 0.f)) * hi2f(tv[m]);
            }
            const u32x4 okv = {pk(kv[0], kv[1]), pk(kv[2], kv[3]), pk(kv[4], kv[5]), pk(kv[6], kv[7])};
            __builtin_amdgcn_raw_buffer_store_b128(okv, sig_rsrc, go, 0, 16);
        }
        stamp(ws, 2);
        {
            float z[2];
#pragma unroll
            for (int u = 0; u < 2; ++u) {
                z[u] = dot2(gq[u][0], gwv[0], 0.f) + dot2(gq[u][1], gwv[1], 0.f);
                z[u] += dot2(gq[u][2], gwv[2], 0.f) + dot2(gq[u][3], gwv[3], 0.f);
            }
            auto h = __builtin_amdgcn_permlane32_swap(__float_as_uint(z[0]), __float_as_uint(z[1]), false, false);
            float y = __uint_as_float(h[0]) + __uint_as_float(h[1]);
            y += __uint_as_float(__builtin_amdgcn_update_dpp(0u, __float_as_uint(y), 0x128, 0xf, 0xf, true));
            y += __uint_as_float(__builtin_amdgcn_update_dpp(0u, __float_as_uint(y), 0x124, 0xf, 0xf, true));
            y += __uint_as_float(__builtin_amdgcn_update_dpp(0u, __float_as_uint(y), 0x122, 0xf, 0xf, true));
            y += __uint_as_float(__builtin_amdgcn_update_dpp(0u, __float_as_uint(y), 0x121, 0xf, 0xf, true));
            { auto s = __builtin_amdgcn_permlane16_swap(__float_as_uint(y), __float_as_uint(y), false, false); y = __uint_as_float(s[0]) + __uint_as_float(s[1]); }
            y = rb(y);
            const float g = __builtin_amdgcn_rcpf(1.f + __builtin_amdgcn_exp2f(y * -1.44269504088896341f));
            if ((lane & 31) == 0) __builtin_amdgcn_raw_buffer_store_b32(pk(g, 0.f), g_rsrc, 4 * (8 * w + 2 * wv + (lane >> 5)), 0, 16);
        }
        {
            asm volatile("s_waitcnt vmcnt(0)" : "+v"(tag) : "v"(pfv) : "memory");
            tag += 1;
            u32 old = 0;
            if (lane == 0) old = __hip_atomic_fetch_add((LAS u32*)(size_t)C_PUB, 1u, __ATOMIC_RELAXED, __HIP_MEMORY_SCOPE_WORKGROUP);
            old = __builtin_amdgcn_readfirstlane(old);
            if (old == 3 && lane == 0) __hip_atomic_store((gu64*)(ws + WS_FLAG1) + (grp * 32 + ti) * FSTR, tag, __ATOMIC_RELAXED, __HIP_MEMORY_SCOPE_AGENT);
        }
        __builtin_amdgcn_s_setprio(0);
        stamp(ws, 4);
        if (wv == 0) {
            __builtin_amdgcn_s_sleep(POLL_DELAY);
            wait_flags((const gu64*)(ws + WS_FLAG1) + (grp * 32 + (lane & 31)) * FSTR, tag);
            lds_arrive(C_GO, lane);
        } else {
            lds_await(C_GO, 1);
            if (wv == 1) {
                const gu64* f = (const gu64*)(ws + WS_FLAG1) + lane * FSTR;
#pragma unroll 1
                for (u32 spins = 0; spins < (1u << 18); ++spins) {
                    const u64 x0 = __hip_atomic_load(f, __ATOMIC_RELAXED, __HIP_MEMORY_SCOPE_AGENT), x1 = __hip_atomic_load(f + 64 * FSTR, __ATOMIC_RELAXED, __HIP_MEMORY_SCOPE_AGENT);
                    const u64 x2 = __hip_atomic_load(f + 128 * FSTR, __ATOMIC_RELAXED, __HIP_MEMORY_SCOPE_AGENT), x3 = __hip_atomic_load(f + 192 * FSTR, __ATOMIC_RELAXED, __HIP_MEMORY_SCOPE_AGENT);
                    if (__all(x0 == tag && x1 == tag && x2 == tag && x3 == tag)) break;
                    __builtin_amdgcn_s_sleep(8);
                }
                lds_arrive(C_GO2, lane);
                if (w == 0 && lane == 0) __hip_atomic_store((gu64*)(ws + WS_EPOCH), tag, __ATOMIC_RELAXED, __HIP_MEMORY_SCOPE_AGENT);
            }
        }
        stamp(ws, 7);
        {
            const u32 go = (((wv & 1) * D + d0 + (wv >> 1)) * S) * 2 + lane * 16;
            const u32x4 v0 = __builtin_amdgcn_raw_buffer_load_b128(sig_rsrc, go, 0, 16), v1 = __builtin_amdgcn_raw_buffer_load_b128(sig_rsrc, go + 1024, 0, 16);
            const u32x4 v2 = __builtin_amdgcn_raw_buffer_load_b128(sig_rsrc, go + 2048, 0, 16), v3 = __builtin_amdgcn_raw_buffer_load_b128(sig_rsrc, go + 3072, 0, 16);
            const int la = L_SIG + wv * SIG_ST + lane * 16;
            lds_st<u32x4>(la, v0); lds_st<u32x4>(la + 1024, v1); lds_st<u32x4>(la + 2048, v2); lds_st<u32x4>(la + 3072, v3);
        }
        stamp(ws, 14);
        lds_await(C_KT, 4);
        stamp(ws, 8);
        const int r = lane & 15, q = lane >> 4;
        const int a = (r + 1) & 3;
        const int ga = ((wv & 1) ? L_X : L_KTA) + (wv >> 1) * KT_CC + a * KT_COPY + 2 * (1 + r + 8 * q - a);
        const int fa = L_SIG + wv * SIG_ST + 2 * (240 - 16 * r + 8 * q);
        const int ya = L_Y + wv * 4096 + 2 * (16 * r + 4 * q);
        f32x4 acc[4];
        conv_all(ga, ga + 8, fa, ya, C_Y + 4 * wv, acc[0], acc[1], acc[2], acc[3]);
        stamp(ws, 9);
#pragma unroll
        for (int ct = 0; ct < 4; ++ct) lds_st<u32x2>(ya + 512 * (4 + ct), (u32x2){pk(acc[ct][0], acc[ct][1]), pk(acc[ct][2], acc[ct][3])});
        __syncthreads();
    } else {
        asm volatile("" : "+v"(pl), "+v"(pa), "+v"(pb), "+v"(pc), "+v"(pd), "+v"(pe), "+v"(pf));
        stamp(ws, 16);
        {
            const float dt = rb(__builtin_amdgcn_exp2f(lo2f(pl) * 1.44269504088896341f));
            const float la = dt * lo2f(pa) * 1.44269504088896341f;
            float rv = dt * lo2f(pb) * 0.15915494309189535f;
            rv -= rintf(rv);
            const u32 tb = L_X + kch * TB_CH + 256 * (kw & 1);
            const cpx w128 = cexp_rev(la * 128.f, rv * 128.f), w1 = cexp_rev(la, rv), u1 = cexp_rev(la * 8.f, rv * -8.f);
            const cpx R0 = cexp_rev(la * 1024.f, __builtin_amdgcn_fractf(rv * 1024.f));
            const float br = dt * lo2f(pc), bi = dt * lo2f(pd), cr = lo2f(pe), ci = lo2f(pf);
            const cpx w256 = cmul(w128, w128), w2 = cmul(w1, w1), u2 = cmul(u1, u1);
            cpx Q[8];
            Q[0] = (cpx){cr * br - ci * bi, -(cr * bi + ci * br)};
            const cpx w3 = cmul(w2, w1), w4 = cmul(w2, w2), u4 = cmul(u2, u2), w384 = cmul(w256, w128), w512 = cmul(w256, w256);
            Q[1] = cmul(Q[0], u1); Q[2] = cmul(Q[0], u2); Q[3] = cmul(Q[1], u2);
            const cpx w5 = cmul(w4, w1), w6 = cmul(w4, w2), w7 = cmul(w4, w3), u8 = cmul(u4, u4);
            st4_addtid<48 * TB_ROW, 49 * TB_ROW, 50 * TB_ROW, 51 * TB_ROW>(tb, pkc(Q[0]), pkc(Q[1]), pkc(Q[2]), pkc(Q[3]));
            st4_addtid<0 * TB_ROW, 1 * TB_ROW, 2 * TB_ROW, 3 * TB_ROW>(tb, 0x3f80u, pkc(w1), pkc(w2), pkc(w3));
#pragma unroll
            for (int b = 0; b < 4; ++b) Q[4 + b] = cmul(Q[b], u4);
            st4_addtid<4 * TB_ROW, 5 * TB_ROW, 6 * TB_ROW, 7 * TB_ROW>(tb, pkc(w4), pkc(w5), pkc(w6), pkc(w7));
            st4_addtid<52 * TB_ROW, 53 * TB_ROW, 54 * TB_ROW, 55 * TB_ROW>(tb, pkc(Q[4]), pkc(Q[5]), pkc(Q[6]), pkc(Q[7]));
            st4_addtid<8 * TB_ROW, 9 * TB_ROW, 10 * TB_ROW, 11 * TB_ROW>(tb, pkc(w128), pkc(cmul(w128, w1)), pkc(cmul(w128, w2)), pkc(cmul(w128, w3)));
            st4_addtid<12 * TB_ROW, 13 * TB_ROW, 14 * TB_ROW, 15 * TB_ROW>(tb, pkc(cmul(w128, w4)), pkc(cmul(w128, w5)), pkc(cmul(w128, w6)), pkc(cmul(w128, w7)));
            st4_addtid<56 * TB_ROW, 57 * TB_ROW, 58 * TB_ROW, 59 * TB_ROW>(tb, pkc(cmul(Q[0], u8)), pkc(cmul(Q[1], u8)), pkc(cmul(Q[2], u8)), pkc(cmul(Q[3], u8)));
            st4_addtid<60 * TB_ROW, 61 * TB_ROW, 62 * TB_ROW, 63 * TB_ROW>(tb, pkc(cmul(Q[4], u8)), pkc(cmul(Q[5], u8)), pkc(cmul(Q[6], u8)), pkc(cmul(Q[7], u8)));
            st4_addtid<16 * TB_ROW, 17 * TB_ROW, 18 * TB_ROW, 19 * TB_ROW>(tb, pkc(w256), pkc(cmul(w256, w2)), pkc(cmul(w256, w4)), pkc(cmul(w256, w6)));
            st4_addtid<20 * TB_ROW, 21 * TB_ROW, 22 * TB_ROW, 23 * TB_ROW>(tb, pkc(w384), pkc(cmul(w384, w2)), pkc(cmul(w384, w4)), pkc(cmul(w384, w6)));
            const cpx z1 = cmul(w512, w128), z2 = cmul(w512, w256), z3 = cmul(w512, w384);
            st4_addtid<24 * TB_ROW, 25 * TB_ROW, 26 * TB_ROW, 27 * TB_ROW>(tb, pkc(w512), pkc(cmul(w512, w4)), pkc(z1), pkc(cmul(z1, w4)));
            st4_addtid<28 * TB_ROW, 29 * TB_ROW, 30 * TB_ROW, 31 * TB_ROW>(tb, pkc(z2), pkc(cmul(z2, w4)), pkc(z3), pkc(cmul(z3, w4)));
            const cpx R1 = cmul(R0, w128), R2 = cmul(R0, w256), R3 = cmul(R1, w256);
            st4_addtid<32 * TB_ROW, 33 * TB_ROW, 34 * TB_ROW, 35 * TB_ROW>(tb, pkc(R0), pkc(R1), pkc(R2), pkc(R3));
            st4_addtid<36 * TB_ROW, 37 * TB_ROW, 38 * TB_ROW, 39 * TB_ROW>(tb, pkc(cmul(R0, w512)), pkc(cmul(R1, w512)), pkc(cmul(R2, w512)), pkc(cmul(R3, w512)));
            lds_st<u32>(tb + 40 * TB_ROW + 4 * lane, pkc(cmul(R0, R0)));
        }
        lds_arrive(C_TB, lane);
        stamp(ws, 3);
#pragma unroll
        for (int ch = 0; ch < 2; ++ch) write_copies(L_KTA + ch * KT_CC, kt, sm[ch][0], sm[ch][1], sm[ch][2], sm[ch][3], sm2[ch][0], kt < 255 ? sm2[ch][1] : 0u);
        if (kw == 0) write_edges(L_KTA, lane, sme[0], sme[1]);
        lds_await(C_TB, 4);
        stamp(ws, 5);
#pragma unroll
        for (int jb = 0; jb < 2; ++jb) {
            const int job = kw + 4 * jb;
            if (job < 6) {
                const int kc = job >= 3, rt = job - 3 * kc, r = lane & 15, q = lane >> 4;
                const int ab = L_X + kc * TB_CH + (16 * rt + r) * TB_ROW + 16 * q, bb = L_X + kc * TB_CH + (48 + r) * TB_ROW + 16 * q;
                u32x4 A[8], B[8];
#pragma unroll
                for (int t = 0; t < 8; ++t) { A[t] = lds_ld<u32x4>(ab + 64 * t); B[t] = lds_ld<u32x4>(bb + 64 * t); }
                asm volatile("" : "+v"(A[0]), "+v"(A[1]), "+v"(A[2]), "+v"(A[3]), "+v"(A[4]), "+v"(A[5]), "+v"(A[6]), "+v"(A[7]),
                                  "+v"(B[0]), "+v"(B[1]), "+v"(B[2]), "+v"(B[3]), "+v"(B[4]), "+v"(B[5]), "+v"(B[6]), "+v"(B[7]));
                f32x4 acc0 = {0, 0, 0, 0}, acc1 = {0, 0, 0, 0};
#pragma unroll
                for (int t = 0; t < 8; t += 2) {
                    acc0 = __builtin_amdgcn_mfma_f32_16x16x32_bf16(__builtin_bit_cast(bf16x8, A[t]), __builtin_bit_cast(bf16x8, B[t]), acc0, 0, 0, 0);
                    acc1 = __builtin_amdgcn_mfma_f32_16x16x32_bf16(__builtin_bit_cast(bf16x8, A[t + 1]), __builtin_bit_cast(bf16x8, B[t + 1]), acc1, 0, 0, 0);
                }
                const u32x2 xt = jb ? xt1 : xt0;
                const int kb = L_KB + kc * 1312;
                lds_st<u16>(kb + (xt[0] & 0xffff), (u16)pk(acc0[0] + acc1[0], 0.f));
                lds_st<u16>(kb + (xt[0] >> 16), (u16)pk(acc0[1] + acc1[1], 0.f));
                lds_st<u16>(kb + (xt[1] & 0xffff), (u16)pk(acc0[2] + acc1[2], 0.f));
                lds_st<u16>(kb + (xt[1] >> 16), (u16)pk(acc0[3] + acc1[3], 0.f));
            }
        }
        lds_arrive(C_KB, lane);
        stamp(ws, 13);
        lds_await(C_KB, 4);
#pragma unroll
        for (int ch = 0; ch < 2; ++ch) {
            const int kb = L_KB + ch * 1312;
            u32 e[11];
#pragma unroll
            for (int m = 0; m < 11; ++m) {
                const u32 t = m < 8 ? gt0[m >> 1] : gt1[(m - 8) >> 1];
                e[m] = lds_ld<u16>(kb + ((m & 1) ? (t >> 16) : (t & 0xffff)));
            }
            write_copies(L_X + ch * KT_CC, kt, e[0] | (e[1] << 16), e[2] | (e[3] << 16), e[4] | (e[5] << 16), e[6] | (e[7] << 16), e[8] | (e[9] << 16), e[10]);
        }
        if (kw == 0) { const u32x2 k03 = lds_ld<u32x2>(L_KB + ((lane >> 2) & 1) * 1312); write_edges(L_X, lane, k03[0], k03[1]); }
        lds_arrive(C_KT, lane);
        stamp(ws, 6);
        lds_await(C_GO2, 1);
        asm volatile("" :: "v"(pfv), "v"(tag));
        stamp(ws, 15);
        const int t4 = 4 * kt;
        u32x4 gg[2]; u32x2 sv[2][4];
#pragma unroll
        for (int h = 0; h < 2; ++h) {
            gg[h] = __builtin_amdgcn_raw_buffer_load_b128(g_rsrc, 4 * t4 + 4096 * h, 0, 16);
#pragma unroll
            for (int sg = 0; sg < 4; ++sg)
                sv[h][sg] = __builtin_bit_cast(u32x2, __builtin_amdgcn_raw_buffer_load_b64(sig_rsrc, (((sg & 1) * D + d0 + (sg >> 1)) * S + 2044 - t4 - 1024 * h) * 2, 0, 16));
        }
        const float sD0 = bf2f(P.sD[d0]), sD1 = bf2f(P.sD[d0 + 1]), dk0 = bf2f(P.dsk[d0]), dk1 = bf2f(P.dsk[d0 + 1]);
        float cA[2][2][4], cB[2][4], c0[2][2][4];
#pragma unroll
        for (int h = 0; h < 2; ++h) {
#pragma unroll
            for (int i = 0; i < 4; ++i) {
                const int ri = 3 - i;
                const float g = lo2f(gg[h][i]), omg = rb(1.f - g);
                cB[h][i] = omg;
#pragma unroll
                for (int ch = 0; ch < 2; ++ch) {
                    const u32x2 kv = sv[h][2 * ch], x = sv[h][2 * ch + 1];
                    const float fkv = (ri & 1) ? hi2f(kv[ri >> 1]) : lo2f(kv[ri >> 1]), fx = (ri & 1) ? hi2f(x[ri >> 1]) : lo2f(x[ri >> 1]);
                    cA[h][ch][i] = fmul(g, fx);
                    c0[h][ch][i] = fmul(fx, ffma(fmul(g, ch ? sD1 : sD0), fkv, fmul(omg, ch ? dk1 : dk0)));
                    asm volatile("" : "+v"(cA[h][ch][i]), "+v"(c0[h][ch][i]));
                }
                asm volatile("" : "+v"(cB[h][i]));
            }
        }
        stamp(ws, 9);
#pragma unroll
        for (int h = 0; h < 2; ++h) {
            if (h == 0) lds_wait4(C_Y, 1); else __syncthreads();
            if (h == 0) stamp(ws, 12);
            const int s0 = t4 + 1024 * h;
            float res[2][4];
#pragma unroll
            for (int ch = 0; ch < 2; ++ch) {
                const u32x2 ya = lds_ld<u32x2>(L_Y + (2 * ch) * 4096 + 2 * s0), yb = lds_ld<u32x2>(L_Y + (2 * ch + 1) * 4096 + 2 * s0);
#pragma unroll
                for (int i = 0; i < 4; ++i) {
                    const float fya = (i & 1) ? hi2f(ya[i >> 1]) : lo2f(ya[i >> 1]), fyb = (i & 1) ? hi2f(yb[i >> 1]) : lo2f(yb[i >> 1]);
                    res[ch][i] = ffma(cA[h][ch][i], fya, ffma(cB[h][i], fyb, c0[h][ch][i]));
                }
            }
#pragma unroll
            for (int i = 0; i < 4; ++i) *(u32*)(P.out + (size_t)(s0 + i) * D + d0) = pk(res[0][i], res[1][i]);
            if (h == 0) stamp(ws, 10);
        }
    }
    stamp(ws, 11);
    STAMP_FLUSH;
    asm volatile(".Lfk_pf_end:");
}

extern "C" void kernel_launch(void* const* d_in, const int* in_sizes, int n_in, void* d_out, int out_size, void* d_ws, size_t ws_size, hipStream_t stream) {
    Params p;
    p.q = (const u16*)d_in[0]; p.k = (const u16*)d_in[1]; p.v = (const u16*)d_in[2]; p.sk = (const u16*)d_in[3];
    p.ssm = (const u16*)d_in[4]; p.sD = (const u16*)d_in[5];
    p.are = (const u16*)d_in[7]; p.aim = (const u16*)d_in[8]; p.bre = (const u16*)d_in[9]; p.bim = (const u16*)d_in[10];
    p.cre = (const u16*)d_in[11]; p.cim = (const u16*)d_in[12]; p.ldt = (const u16*)d_in[13]; p.dsk = (const u16*)d_in[14]; p.gw = (const u16*)d_in[15];
    p.out = (u16*)d_out; p.ws = (char*)d_ws;
    (void)hipFuncSetAttribute((const void*)fused_kernel, hipFuncAttributeMaxDynamicSharedMemorySize, L_END);
    fused_kernel<<<NWG, NT, L_END, stream>>>(p);
}
```

```cpp
#define DELAY_AT 1
#define DELAY_US 3
#include <hip/hip_runtime.h>
#include <stdint.h>

typedef unsigned short u16;
typedef unsigned int u32;
typedef unsigned long long u64;
typedef u32 u32x4 __attribute__((ext_vector_type(4)));
typedef u32 u32x2 __attribute__((ext_vector_type(2)));
typedef float f32x4 __attribute__((ext_vector_type(4)));
typedef float f32x2 __attribute__((ext_vector_type(2)));
typedef __bf16 bf16x2 __attribute__((ext_vector_type(2)));
typedef __bf16 bf16x8 __attribute__((ext_vector_type(8)));
#define GAS __attribute__((address_space(1)))
#define LAS __attribute__((address_space(3)))
typedef GAS u64 gu64;

#define FSTR 16
#define POLL_DELAY 2
#define POLL_GAP 1
#define STAMPS 0

constexpr int S = 2048, D = 512, NS = 128;
constexpr int NWG = 256, NT = 512;

constexpr size_t WS_EPOCH = 0;
constexpr size_t WS_FLAG1 = 4096;
constexpr size_t WS_G     = 49152;
constexpr size_t WS_SIG   = 65536;
constexpr size_t WS_STAMP = 8u << 20;

constexpr int KT_COPY = 4160;
constexpr int KT_CC   = 4 * KT_COPY;
constexpr int TB_ROW  = 528, TB_CH = 64 * TB_ROW;
constexpr int L_X     = 0;
constexpr int L_Y     = L_X + 2 * KT_CC;
constexpr int L_KTA   = L_X + 2 * TB_CH;
constexpr int SIG_ST  = 4608;
constexpr int L_SIG   = L_KTA + 2 * KT_CC;
constexpr int L_T     = L_SIG + 4 * SIG_ST;
constexpr int L_H     = L_T + 3 * 8192;
constexpr int L_KB    = L_H + 1024;
constexpr int L_MISC  = L_KB + 2 * 1312;
constexpr int L_END   = L_MISC + 64;
static_assert(L_Y + 16384 <= L_KTA, "Y overlay");

__device__ __forceinline__ u32 pk(float lo, float hi) { f32x2 v = {lo, hi}; bf16x2 b = __builtin_convertvector(v, bf16x2); return __builtin_bit_cast(u32, b); }
__device__ __forceinline__ float rb(float x) { return __uint_as_float(pk(0.f, x)); }
__device__ __forceinline__ float lo2f(u32 d) { return __uint_as_float(d << 16); }
__device__ __forceinline__ float hi2f(u32 d) { return __uint_as_float(d & 0xffff0000u); }
__device__ __forceinline__ float bf2f(u16 v) { return __uint_as_float(((u32)v) << 16); }

template <typename T> __device__ __forceinline__ T lds_ld(int off) { return *(const LAS T*)(size_t)(u32)off; }
template <typename T> __device__ __forceinline__ void lds_st(int off, T v) { *(LAS T*)(size_t)(u32)off = v; }

struct IdxTabs { u16 g[256][12]; u16 x[3][64][4]; };
constexpr int bf16_round_int(int l) {
    int sh = 0; while ((l >> sh) >= 256) ++sh;
    if (sh == 0) return l;
    const int q = l >> sh, rem = l & ((1 << sh) - 1), half = 1 << (sh - 1);
    const int up = rem > half || (rem == half && (q & 1));
    return (q + up) << sh;
}
constexpr int kt_index(int l) {
    const int v = bf16_round_int(l);
    return v < 256 ? v : v < 512 ? 256 + (v - 256) / 2 : v < 1024 ? 384 + (v - 512) / 4 : 512 + (v - 1024) / 8;
}
constexpr IdxTabs make_tabs() {
    IdxTabs t{};
    for (int j = 0; j < 256; ++j)
        for (int m = 0; m < 12; ++m) { int l = 8 * j + m; if (l > 2047) l = 2047; t.g[j][m] = (u16)(2 * kt_index(l)); }
    for (int rt = 0; rt < 3; ++rt)
        for (int lane = 0; lane < 64; ++lane)
            for (int i = 0; i < 4; ++i) {
                const int row = 4 * (lane >> 4) + i, b = lane & 15;
                int idx = 0;
                if (rt == 0) idx = (row < 8 ? row : 120 + row) + 8 * b;
                else if (rt == 1) idx = row < 8 ? 256 + 64 * (row >> 2) + (row & 3) + 4 * b : 384 + 32 * ((row - 8) >> 1) + (row & 1) + 2 * b;
                else idx = row < 8 ? 512 + 16 * row + b : ((row == 8 && b == 0) ? 640 : 648);
                t.x[rt][lane][i] = (u16)(2 * idx);
            }
    return t;
}
__device__ static const IdxTabs idx_tabs = make_tabs();

struct Params {
    const u16 *q, *k, *v, *sk, *ssm, *sD, *are, *aim, *bre, *bim, *cre, *cim, *ldt, *dsk, *gw;
    u16* out; char* ws;
};

#define stamp(ws, slot) do {} while (0)
#define STAMP_DECL
#define STAMP_FLUSH do {} while (0)

template <int N>
__device__ __forceinline__ void tr8n(u32x4 (&d)[N], u32 sel) {
    u32 e[N][4];
#pragma unroll
    for (int n = 0; n < N; ++n)
#pragma unroll
        for (int m = 0; m < 4; ++m) e[n][m] = __builtin_amdgcn_update_dpp(0u, d[n][m], 0x128, 0xf, 0xf, true);
#pragma unroll
    for (int n = 0; n < N; ++n)
#pragma unroll
        for (int m = 0; m < 4; ++m) e[n][m] = __builtin_amdgcn_perm(e[n][m], d[n][m], sel);
#pragma unroll
    for (int n = 0; n < N; ++n) {
        auto a = __builtin_amdgcn_permlane16_swap(e[n][0], e[n][1], false, false);
        auto b = __builtin_amdgcn_permlane16_swap(e[n][2], e[n][3], false, false);
        e[n][0] = a[0]; e[n][1] = a[1]; e[n][2] = b[0]; e[n][3] = b[1];
    }
#pragma unroll
    for (int n = 0; n < N; ++n) {
        auto c = __builtin_amdgcn_permlane32_swap(e[n][0], e[n][2], false, false);
        auto f = __builtin_amdgcn_permlane32_swap(e[n][1], e[n][3], false, false);
        d[n] = (u32x4){f[1], c[1], f[0], c[0]};
    }
}

__device__ __forceinline__ void write_copies(int base, int j, u32 d0, u32 d1, u32 d2, u32 d3, u32 d4, u32 d5) {
    u32 a10 = __builtin_amdgcn_alignbit(d1, d0, 16), a21 = __builtin_amdgcn_alignbit(d2, d1, 16), a32 = __builtin_amdgcn_alignbit(d3, d2, 16),
        a43 = __builtin_amdgcn_alignbit(d4, d3, 16), a54 = __builtin_amdgcn_alignbit(d5, d4, 16);
    int o = base + 32 + 16 * j;
    lds_st<u32x4>(o, (u32x4){d0, d1, d2, d3});
    lds_st<u32x4>(o + KT_COPY, (u32x4){a10, a21, a32, a43});
    lds_st<u32x4>(o + 2 * KT_COPY, (u32x4){d1, d2, d3, d4});
    lds_st<u32x4>(o + 3 * KT_COPY, (u32x4){a21, a32, a43, a54});
}
__device__ __forceinline__ void write_edges(int base, int lane, u32 k01, u32 k23) {
    const int a = lane & 3, ch = (lane >> 2) & 1, z = lane >> 3;
    u32 d2 = a == 3 ? k01 << 16 : 0u;
    u32 d3 = a == 0 ? 0u : a == 1 ? k01 << 16 : a == 2 ? k01 : __builtin_amdgcn_alignbit(k23, k01, 16);
    if (z) { d2 = 0u; d3 = 0u; }
    if (lane < 16) lds_st<u32x4>(base + ch * KT_CC + a * KT_COPY + (z ? 0 : 16), (u32x4){0u, 0u, d2, d3});
}
__device__ __forceinline__ void lds_arrive(int addr, int lane) {
    asm volatile("" ::: "memory");
    if (lane == 0) __hip_atomic_fetch_add((LAS u32*)(size_t)(u32)addr, 1u, __ATOMIC_RELAXED, __HIP_MEMORY_SCOPE_WORKGROUP);
}
__device__ __forceinline__ void lds_await(int addr, u32 n) {
#pragma unroll 1
    for (u32 spins = 0; spins < (1u << 24); ++spins)
        if (*(const volatile LAS u32*)(size_t)(u32)addr >= n) break;
    asm volatile("" ::: "memory");
}

__device__ __forceinline__ bool wait_flags(const gu64* f, u64 tag) {
#pragma unroll 1
    for (u32 spins = 0; spins < (1u << 18); ++spins) {
        u64 x = __hip_atomic_load(f, __ATOMIC_RELAXED, __HIP_MEMORY_SCOPE_AGENT);
        if (__all(x == tag)) return true;
        __builtin_amdgcn_s_sleep(POLL_GAP);
    }
    return false;
}

typedef f32x2 cpx;
__device__ __forceinline__ cpx cmul(cpx a, cpx b) {
    cpx r;
    asm("v_pk_mul_f32 %0, %1, %2 op_sel_hi:[0,1]\n\tv_pk_fma_f32 %0, %1, %2, %0 op_sel:[1,1,0] op_sel_hi:[1,0,1] neg_lo:[1,0,0]" : "=&v"(r) : "v"(a), "v"(b));
    return r;
}
__device__ __forceinline__ cpx cexp_rev(float lm, float rev) {
    const float e = __builtin_amdgcn_exp2f(lm);
    return (cpx){e * __builtin_amdgcn_cosf(rev), e * __builtin_amdgcn_sinf(rev)};
}
__device__ __forceinline__ u32 pkc(cpx a) { return pk(a.x, a.y); }
__device__ __forceinline__ float ffma(float a, float b, float c) { float r; asm("v_fma_f32 %0, %1, %2, %3" : "=v"(r) : "v"(a), "v"(b), "v"(c)); return r; }
__device__ __forceinline__ float fmul(float a, float b) { float r; asm("v_mul_f32 %0, %1, %2" : "=v"(r) : "v"(a), "v"(b)); return r; }
template <int O0, int O1, int O2, int O3>
__device__ __forceinline__ void st4_addtid(u32 base, u32 a, u32 b, u32 c, u32 d) {
    u32 keep;
    asm volatile("s_mov_b32 %0, m0\n\ts_mov_b32 m0, %1\n\ts_nop 0\n\t"
                 "ds_write_addtid_b32 %2 offset:%6\n\tds_write_addtid_b32 %3 offset:%7\n\tds_write_addtid_b32 %4 offset:%8\n\tds_write_addtid_b32 %5 offset:%9\n\t"
                 "s_mov_b32 m0, %0"
                 : "=&s"(keep) : "s"(base), "v"(a), "v"(b), "v"(c), "v"(d), "i"(O0), "i"(O1), "i"(O2), "i"(O3) : "memory");
}

__device__ __forceinline__ void conv_all(int ga, int gb, int fa, int ya, int yf, f32x4& a4, f32x4& a5, f32x4& a6, f32x4& a7) {
    u32 cnt;
    asm volatile(
        "s_setprio 3\n"
        "ds_read_b64 v[128:129], %[ga] offset:0\n"
        "ds_read_b64 v[130:131], %[gb] offset:0\n"
        "ds_read_b128 v[144:147], %[fa] offset:3584\n"
        "ds_read_b128 v[148:151], %[fa] offset:3072\n"
        "ds_read_b128 v[152:155], %[fa] offset:2560\n"
        "ds_read_b128 v[156:159], %[fa] offset:2048\n"
        "ds_read_b64 v[132:133], %[ga] offset:512\n"
        "ds_read_b64 v[134:135], %[gb] offset:512\n"
        "ds_read_b64 v[136:137], %[ga] offset:1024\n"
        "ds_read_b64 v[138:139], %[gb] offset:1024\n"
        "ds_read_b64 v[140:141], %[ga] offset:1536\n"
        "ds_read_b64 v[142:143], %[gb] offset:1536\n"
        "ds_read_b64 v[160:161], %[ga] offset:64\n"
        "ds_read_b64 v[162:163], %[gb] offset:64\n"
        "ds_read_b128 v[176:179], %[fa] offset:3648\n"
        "ds_read_b128 v[180:183], %[fa] offset:3136\n"
        "ds_read_b128 v[184:187], %[fa] offset:2624\n"
        "ds_read_b128 v[188:191], %[fa] offset:2112\n"
        "ds_read_b64 v[164:165], %[ga] offset:576\n"
        "ds_read_b64 v[166:167], %[gb] offset:576\n"
        "ds_read_b64 v[168:169], %[ga] offset:1088\n"
        "ds_read_b64 v[170:171], %[gb] offset:1088\n"
        "ds_read_b64 v[172:173], %[ga] offset:1600\n"
        "ds_read_b64 v[174:175], %[gb] offset:1600\n"
        "s_waitcnt lgkmcnt(15)\n"
        "v_mfma_f32_16x16x32_bf16 v[112:115], v[128:131], v[144:147], 0\n"
        "ds_read_b64 v[192:193], %[ga] offset:128\n"
        "ds_read_b64 v[194:195], %[gb] offset:128\n"
        "v_mfma_f32_16x16x32_bf16 v[116:119], v[128:131], v[148:151], 0\n"
        "ds_read_b128 v[208:211], %[fa] offset:3712\n"
        "v_mfma_f32_16x16x32_bf16 v[120:123], v[128:131], v[152:155], 0\n"
        "ds_read_b128 v[212:215], %[fa] offset:3200\n"
        "v_mfma_f32_16x16x32_bf16 v[124:127], v[128:131], v[156:159], 0\n"
        "ds_read_b128 v[216:219], %[fa] offset:2688\n"
        "ds_read_b128 v[220:223], %[fa] offset:2176\n"
        "v_mfma_f32_16x16x32_bf16 v[116:119], v[132:135], v[144:147], v[116:119]\n"
        "ds_read_b64 v[196:197], %[ga] offset:640\n"
        "v_mfma_f32_16x16x32_bf16 v[120:123], v[132:135], v[148:151], v[120:123]\n"
        "ds_read_b64 v[198:199], %[gb] offset:640\n"
        "v_mfma_f32_16x16x32_bf16 v[124:127], v[132:135], v[152:155], v[124:127]\n"
        "ds_read_b64 v[200:201], %[ga] offset:1152\n"
        "ds_read_b64 v[202:203], %[gb] offset:1152\n"
        "s_waitcnt lgkmcnt(15)\n"
        "v_mfma_f32_16x16x32_bf16 v[120:123], v[136:139], v[144:147], v[120:123]\n"
        "ds_read_b64 v[204:205], %[ga] offset:1664\n"
        "v_mfma_f32_16x16x32_bf16 v[124:127], v[136:139], v[148:151], v[124:127]\n"
        "ds_read_b64 v[206:207], %[gb] offset:1664\n"
        "v_mfma_f32_16x16x32_bf16 v[124:127], v[140:143], v[144:147], v[124:127]\n"
        "ds_read_b64 v[224:225], %[ga] offset:192\n"
        "ds_read_b64 v[226:227], %[gb] offset:192\n"
        "v_mfma_f32_16x16x32_bf16 v[112:115], v[160:163], v[176:179], v[112:115]\n"
        "ds_read_b128 v[240:243], %[fa] offset:3776\n"
        "v_mfma_f32_16x16x32_bf16 v[116:119], v[160:163], v[180:183], v[116:119]\n"
        "ds_read_b128 v[244:247], %[fa] offset:3264\n"
        "v_mfma_f32_16x16x32_bf16 v[120:123], v[160:163], v[184:187], v[120:123]\n"
        "ds_read_b128 v[248:251], %[fa] offset:2752\n"
        "ds_read_b128 v[252:255], %[fa] offset:2240\n"
        "v_mfma_f32_16x16x32_bf16 v[124:127], v[160:163], v[188:191], v[124:127]\n"
        "ds_read_b64 v[228:229], %[ga] offset:704\n"
        "s_waitcnt lgkmcnt(15)\n"
        "v_mfma_f32_16x16x32_bf16 v[116:119], v[164:167], v[176:179], v[116:119]\n"
        "ds_read_b64 v[230:231], %[gb] offset:704\n"
        "v_mfma_f32_16x16x32_bf16 v[120:123], v[164:167], v[180:183], v[120:123]\n"
        "ds_read_b64 v[232:233], %[ga] offset:1216\n"
        "ds_read_b64 v[234:235], %[gb] offset:1216\n"
        "v_mfma_f32_16x16x32_bf16 v[124:127], v[164:167], v[184:187], v[124:127]\n"
        "ds_read_b64 v[236:237], %[ga] offset:1728\n"
        "v_mfma_f32_16x16x32_bf16 v[120:123], v[168:171], v[176:179], v[120:123]\n"
        "ds_read_b64 v[238:239], %[gb] offset:1728\n"
        "v_mfma_f32_16x16x32_bf16 v[124:127], v[168:171], v[180:183], v[124:127]\n"
        "v_mfma_f32_16x16x32_bf16 v[124:127], v[172:175], v[176:179], v[124:127]\n"
        "v_mfma_f32_16x16x32_bf16 v[112:115], v[192:195], v[208:211], v[112:115]\n"
        "ds_read_b64 v[128:129], %[ga] offset:256\n"
        "ds_read_b64 v[130:131], %[gb] offset:256\n"
        "v_mfma_f32_16x16x32_bf16 v[116:119], v[192:195], v[212:215], v[116:119]\n"
        "ds_read_b128 v[144:147], %[fa] offset:3840\n"
        "s_waitcnt lgkmcnt(15)\n"
        "v_mfma_f32_16x16x32_bf16 v[120:123], v[192:195], v[216:219], v[120:123]\n"
        "ds_read_b128 v[148:151], %[fa] offset:3328\n"
        "v_mfma_f32_16x16x32_bf16 v[124:127], v[192:195], v[220:223], v[124:127]\n"
        "ds_read_b128 v[152:155], %[fa] offset:2816\n"
        "ds_read_b128 v[156:159], %[fa] offset:2304\n"
        "v_mfma_f32_16x16x32_bf16 v[116:119], v[196:199], v[208:211], v[116:119]\n"
        "ds_read_b64 v[132:133], %[ga] offset:768\n"
        "v_mfma_f32_16x16x32_bf16 v[120:123], v[196:199], v[212:215], v[120:123]\n"
        "ds_read_b64 v[134:135], %[gb] offset:768\n"
        "v_mfma_f32_16x16x32_bf16 v[124:127], v[196:199], v[216:219], v[124:127]\n"
        "ds_read_b64 v[136:137], %[ga] offset:1280\n"
        "ds_read_b64 v[138:139], %[gb] offset:1280\n"
        "v_mfma_f32_16x16x32_bf16 v[120:123], v[200:203], v[208:211], v[120:123]\n"
        "ds_read_b64 v[140:141], %[ga] offset:1792\n"
        "v_mfma_f32_16x16x32_bf16 v[124:127], v[200:203], v[212:215], v[124:127]\n"
        "ds_read_b64 v[142:143], %[gb] offset:1792\n"
        "v_mfma_f32_16x16x32_bf16 v[124:127], v[204:207], v[208:211], v[124:127]\n"
        "ds_read_b64 v[160:161], %[ga] offset:320\n"
        "ds_read_b64 v[162:163], %[gb] offset:320\n"
        "s_waitcnt lgkmcnt(15)\n"
        "v_mfma_f32_16x16x32_bf16 v[112:115], v[224:227], v[240:243], v[112:115]\n"
        "ds_read_b128 v[176:179], %[fa] offset:3904\n"
        "v_mfma_f32_16x16x32_bf16 v[116:119], v[224:227], v[244:247], v[116:119]\n"
        "ds_read_b128 v[180:183], %[fa] offset:3392\n"
        "v_mfma_f32_16x16x32_bf16 v[120:123], v[224:227], v[248:251], v[120:123]\n"
        "ds_read_b128 v[184:187], %[fa] offset:2880\n"
        "ds_read_b128 v[188:191], %[fa] offset:2368\n"
        "v_mfma_f32_16x16x32_bf16 v[124:127], v[224:227], v[252:255], v[124:127]\n"
        "ds_read_b64 v[164:165], %[ga] offset:832\n"
        "v_mfma_f32_16x16x32_bf16 v[116:119], v[228:231], v[240:243], v[116:119]\n"
        "ds_read_b64 v[166:167], %[gb] offset:832\n"
        "v_mfma_f32_16x16x32_bf16 v[120:123], v[228:231], v[244:247], v[120:123]\n"
        "ds_read_b64 v[168:169], %[ga] offset:1344\n"
        "ds_read_b64 v[170:171], %[gb] offset:1344\n"
        "v_mfma_f32_16x16x32_bf16 v[124:127], v[228:231], v[248:251], v[124:127]\n"
        "ds_read_b64 v[172:173], %[ga] offset:1856\n"
        "v_mfma_f32_16x16x32_bf16 v[120:123], v[232:235], v[240:243], v[120:123]\n"
        "ds_read_b64 v[174:175], %[gb] offset:1856\n"
        "v_mfma_f32_16x16x32_bf16 v[124:127], v[232:235], v[244:247], v[124:127]\n"
        "s_waitcnt lgkmcnt(15)\n"
        "v_mfma_f32_16x16x32_bf16 v[124:127], v[236:239], v[240:243], v[124:127]\n"
        "v_mfma_f32_16x16x32_bf16 v[112:115], v[128:131], v[144:147], v[112:115]\n"
        "ds_read_b64 v[192:193], %[ga] offset:384\n"
        "ds_read_b64 v[194:195], %[gb] offset:384\n"
        "v_mfma_f32_16x16x32_bf16 v[116:119], v[128:131], v[148:151], v[116:119]\n"
        "ds_read_b128 v[208:211], %[fa] offset:3968\n"
        "v_mfma_f32_16x16x32_bf16 v[120:123], v[128:131], v[152:155], v[120:123]\n"
        "ds_read_b128 v[212:215], %[fa] offset:3456\n"
        "v_mfma_f32_16x16x32_bf16 v[124:127], v[128:131], v[156:159], v[124:127]\n"
        "ds_read_b128 v[216:219], %[fa] offset:2944\n"
        "ds_read_b128 v[220:223], %[fa] offset:2432\n"
        "v_mfma_f32_16x16x32_bf16 v[116:119], v[132:135], v[144:147], v[116:119]\n"
        "ds_read_b64 v[196:197], %[ga] offset:896\n"
        "v_mfma_f32_16x16x32_bf16 v[120:123], v[132:135], v[148:151], v[120:123]\n"
        "ds_read_b64 v[198:199], %[gb] offset:896\n"
        "v_mfma_f32_16x16x32_bf16 v[124:127], v[132:135], v[152:155], v[124:127]\n"
        "ds_read_b64 v[200:201], %[ga] offset:1408\n"
        "ds_read_b64 v[202:203], %[gb] offset:1408\n"
        "s_waitcnt lgkmcnt(15)\n"
        "v_mfma_f32_16x16x32_bf16 v[120:123], v[136:139], v[144:147], v[120:123]\n"
        "ds_read_b64 v[204:205], %[ga] offset:1920\n"
        "v_mfma_f32_16x16x32_bf16 v[124:127], v[136:139], v[148:151], v[124:127]\n"
        "ds_read_b64 v[206:207], %[gb] offset:1920\n"
        "v_mfma_f32_16x16x32_bf16 v[124:127], v[140:143], v[144:147], v[124:127]\n"
        "ds_read_b64 v[224:225], %[ga] offset:448\n"
        "ds_read_b64 v[226:227], %[gb] offset:448\n"
        "v_mfma_f32_16x16x32_bf16 v[112:115], v[160:163], v[176:179], v[112:115]\n"
        "ds_read_b128 v[240:243], %[fa] offset:4032\n"
        "v_mfma_f32_16x16x32_bf16 v[116:119], v[160:163], v[180:183], v[116:119]\n"
        "ds_read_b128 v[244:247], %[fa] offset:3520\n"
        "v_mfma_f32_16x16x32_bf16 v[120:123], v[160:163], v[184:187], v[120:123]\n"
        "ds_read_b128 v[248:251], %[fa] offset:3008\n"
        "ds_read_b128 v[252:255], %[fa] offset:2496\n"
        "v_mfma_f32_16x16x32_bf16 v[124:127], v[160:163], v[188:191], v[124:127]\n"
        "ds_read_b64 v[228:229], %[ga] offset:960\n"
        "s_waitcnt lgkmcnt(15)\n"
        "v_mfma_f32_16x16x32_bf16 v[116:119], v[164:167], v[176:179], v[116:119]\n"
        "ds_read_b64 v[230:231], %[gb] offset:960\n"
        "v_mfma_f32_16x16x32_bf16 v[120:123], v[164:167], v[180:183], v[120:123]\n"
        "ds_read_b64 v[232:233], %[ga] offset:1472\n"
        "ds_read_b64 v[234:235], %[gb] offset:1472\n"
        "v_mfma_f32_16x16x32_bf16 v[124:127], v[164:167], v[184:187], v[124:127]\n"
        "ds_read_b64 v[236:237], %[ga] offset:1984\n"
        "v_mfma_f32_16x16x32_bf16 v[120:123], v[168:171], v[176:179], v[120:123]\n"
        "ds_read_b64 v[238:239], %[gb] offset:1984\n"
        "v_mfma_f32_16x16x32_bf16 v[124:127], v[168:171], v[180:183], v[124:127]\n"
        "v_mfma_f32_16x16x32_bf16 v[124:127], v[172:175], v[176:179], v[124:127]\n"
        "v_mfma_f32_16x16x32_bf16 v[112:115], v[192:195], v[208:211], v[112:115]\n"
        "ds_read_b64 v[128:129], %[ga] offset:0\n"
        "ds_read_b64 v[130:131], %[gb] offset:0\n"
        "v_mfma_f32_16x16x32_bf16 v[116:119], v[192:195], v[212:215], v[116:119]\n"
        "ds_read_b128 v[176:179], %[fa] offset:1536\n"
        "s_waitcnt lgkmcnt(15)\n"
        "v_mfma_f32_16x16x32_bf16 v[120:123], v[192:195], v[216:219], v[120:123]\n"
        "ds_read_b128 v[180:183], %[fa] offset:1024\n"
        "v_mfma_f32_16x16x32_bf16 v[124:127], v[192:195], v[220:223], v[124:127]\n"
        "ds_read_b128 v[184:187], %[fa] offset:512\n"
        "ds_read_b128 v[188:191], %[fa] offset:0\n"
        "v_mfma_f32_16x16x32_bf16 v[116:119], v[196:199], v[208:211], v[116:119]\n"
        "ds_read_b64 v[132:133], %[ga] offset:512\n"
        "v_mfma_f32_16x16x32_bf16 v[120:123], v[196:199], v[212:215], v[120:123]\n"
        "ds_read_b64 v[134:135], %[gb] offset:512\n"
        "v_mfma_f32_16x16x32_bf16 v[124:127], v[196:199], v[216:219], v[124:127]\n"
        "ds_read_b128 v[172:175], %[fa] offset:2048\n"
        "ds_read_b64 v[136:137], %[ga] offset:1024\n"
        "v_mfma_f32_16x16x32_bf16 v[120:123], v[200:203], v[208:211], v[120:123]\n"
        "ds_read_b64 v[138:139], %[gb] offset:1024\n"
        "v_mfma_f32_16x16x32_bf16 v[124:127], v[200:203], v[212:215], v[124:127]\n"
        "ds_read_b128 v[168:171], %[fa] offset:2560\n"
        "v_mfma_f32_16x16x32_bf16 v[124:127], v[204:207], v[208:211], v[124:127]\n"
        "ds_read_b64 v[140:141], %[ga] offset:1536\n"
        "ds_read_b64 v[142:143], %[gb] offset:1536\n"
        "s_waitcnt lgkmcnt(15)\n"
        "v_mfma_f32_16x16x32_bf16 v[112:115], v[224:227], v[240:243], v[112:115]\n"
        "ds_read_b128 v[164:167], %[fa] offset:3072\n"
        "v_mfma_f32_16x16x32_bf16 v[116:119], v[224:227], v[244:247], v[116:119]\n"
        "ds_read_b64 v[144:145], %[ga] offset:2048\n"
        "v_mfma_f32_16x16x32_bf16 v[120:123], v[224:227], v[248:251], v[120:123]\n"
        "ds_read_b64 v[146:147], %[gb] offset:2048\n"
        "ds_read_b128 v[160:163], %[fa] offset:3584\n"
        "v_mfma_f32_16x16x32_bf16 v[124:127], v[224:227], v[252:255], v[124:127]\n"
        "ds_read_b64 v[148:149], %[ga] offset:2560\n"
        "v_mfma_f32_16x16x32_bf16 v[116:119], v[228:231], v[240:243], v[116:119]\n"
        "ds_read_b64 v[150:151], %[gb] offset:2560\n"
        "v_mfma_f32_16x16x32_bf16 v[120:123], v[228:231], v[244:247], v[120:123]\n"
        "ds_read_b64 v[152:153], %[ga] offset:3072\n"
        "ds_read_b64 v[154:155], %[gb] offset:3072\n"
        "v_mfma_f32_16x16x32_bf16 v[124:127], v[228:231], v[248:251], v[124:127]\n"
        "ds_read_b64 v[156:157], %[ga] offset:3584\n"
        "v_mfma_f32_16x16x32_bf16 v[120:123], v[232:235], v[240:243], v[120:123]\n"
        "ds_read_b64 v[158:159], %[gb] offset:3584\n"
        "v_mfma_f32_16x16x32_bf16 v[124:127], v[232:235], v[244:247], v[124:127]\n"
        "s_waitcnt lgkmcnt(15)\n"
        "v_mfma_f32_16x16x32_bf16 v[124:127], v[236:239], v[240:243], v[124:127]\n"
        "v_mfma_f32_16x16x32_bf16 %[a4], v[128:131], v[176:179], 0\n"
        "ds_read_b64 v[192:193], %[ga] offset:64\n"
        "v_mfma_f32_16x16x32_bf16 %[a5], v[128:131], v[180:183], 0\n"
        "ds_read_b64 v[194:195], %[gb] offset:64\n"
        "v_mfma_f32_16x16x32_bf16 %[a6], v[128:131], v[184:187], 0\n"
        "ds_read_b128 v[240:243], %[fa] offset:1600\n"
        "v_mfma_f32_16x16x32_bf16 %[a7], v[128:131], v[188:191], 0\n"
        "ds_read_b128 v[244:247], %[fa] offset:1088\n"
        "v_mfma_f32_16x16x32_bf16 %[a7], v[132:135], v[184:187], %[a7]\n"
        "ds_read_b128 v[248:251], %[fa] offset:576\n"
        "v_mfma_f32_16x16x32_bf16 %[a6], v[132:135], v[180:183], %[a6]\n"
        "ds_read_b128 v[252:255], %[fa] offset:64\n"
        "v_mfma_f32_16x16x32_bf16 %[a5], v[132:135], v[176:179], %[a5]\n"
        "ds_read_b64 v[196:197], %[ga] offset:576\n"
        "v_mfma_f32_16x16x32_bf16 %[a4], v[132:135], v[172:175], %[a4]\n"
        "ds_read_b64 v[198:199], %[gb] offset:576\n"
        "s_waitcnt lgkmcnt(15)\n"
        "v_mfma_f32_16x16x32_bf16 %[a7], v[136:139], v[180:183], %[a7]\n"
        "ds_read_b128 v[236:239], %[fa] offset:2112\n"
        "v_cvt_pk_bf16_f32 v104, v112, v113\n"
        "v_cvt_pk_bf16_f32 v105, v114, v115\n"
        "v_mfma_f32_16x16x32_bf16 %[a6], v[136:139], v[176:179], %[a6]\n"
        "ds_read_b64 v[200:201], %[ga] offset:1088\n"
        "ds_write_b64 %[ya], v[104:105] offset:0\n"
        "v_mfma_f32_16x16x32_bf16 %[a5], v[136:139], v[172:175], %[a5]\n"
        "ds_read_b64 v[202:203], %[gb] offset:1088\n"
        "v_cvt_pk_bf16_f32 v106, v116, v117\n"
        "v_cvt_pk_bf16_f32 v107, v118, v119\n"
        "v_mfma_f32_16x16x32_bf16 %[a4], v[136:139], v[168:171], %[a4]\n"
        "ds_read_b128 v[232:235], %[fa] offset:2624\n"
        "ds_write_b64 %[ya], v[106:107] offset:512\n"
        "v_mfma_f32_16x16x32_bf16 %[a7], v[140:143], v[176:179], %[a7]\n"
        "ds_read_b64 v[204:205], %[ga] offset:1600\n"
        "v_cvt_pk_bf16_f32 v108, v120, v121\n"
        "v_cvt_pk_bf16_f32 v109, v122, v123\n"
        "v_mfma_f32_16x16x32_bf16 %[a6], v[140:143], v[172:175], %[a6]\n"
        "ds_read_b64 v[206:207], %[gb] offset:1600\n"
        "ds_write_b64 %[ya], v[108:109] offset:1024\n"
        "v_mfma_f32_16x16x32_bf16 %[a5], v[140:143], v[168:171], %[a5]\n"
        "ds_read_b128 v[228:231], %[fa] offset:3136\n"
        "v_cvt_pk_bf16_f32 v110, v124, v125\n"
        "v_cvt_pk_bf16_f32 v111, v126, v127\n"
        "v_mfma_f32_16x16x32_bf16 %[a4], v[140:143], v[164:167], %[a4]\n"
        "ds_read_b64 v[208:209], %[ga] offset:2112\n"
        "ds_write_b64 %[ya], v[110:111] offset:1536\n"
        "v_mfma_f32_16x16x32_bf16 %[a7], v[144:147], v[172:175], %[a7]\n"
        "ds_read_b64 v[210:211], %[gb] offset:2112\n"
        "v_mfma_f32_16x16x32_bf16 %[a6], v[144:147], v[168:171], %[a6]\n"
        "ds_read_b128 v[224:227], %[fa] offset:3648\n"
        "s_mov_b64 exec, 1\n"
        "ds_write_b32 %[yf], %[one]\n"
        "s_mov_b64 exec, -1\n"
        "v_mfma_f32_16x16x32_bf16 %[a5], v[144:147], v[164:167], %[a5]\n"
        "ds_read_b64 v[212:213], %[ga] offset:2624\n"
        "s_waitcnt lgkmcnt(15)\n"
        "v_mfma_f32_16x16x32_bf16 %[a4], v[144:147], v[160:163], %[a4]\n"
        "ds_read_b64 v[214:215], %[gb] offset:2624\n"
        "v_mfma_f32_16x16x32_bf16 %[a5], v[148:151], v[160:163], %[a5]\n"
        "ds_read_b64 v[216:217], %[ga] offset:3136\n"
        "v_mfma_f32_16x16x32_bf16 %[a6], v[148:151], v[164:167], %[a6]\n"
        "ds_read_b64 v[218:219], %[gb] offset:3136\n"
        "v_mfma_f32_16x16x32_bf16 %[a7], v[148:151], v[168:171], %[a7]\n"
        "ds_read_b64 v[220:221], %[ga] offset:3648\n"
        "v_mfma_f32_16x16x32_bf16 %[a6], v[152:155], v[160:163], %[a6]\n"
        "ds_read_b64 v[222:223], %[gb] offset:3648\n"
        "v_mfma_f32_16x16x32_bf16 %[a7], v[152:155], v[164:167], %[a7]\n"
        "v_mfma_f32_16x16x32_bf16 %[a7], v[156:159], v[160:163], %[a7]\n"
        "s_waitcnt lgkmcnt(8)\n"
        "s_mov_b32 %[cnt], 3\n"
        "1:\n"
        "v_mfma_f32_16x16x32_bf16 %[a4], v[192:195], v[240:243], %[a4]\n"
        "ds_read_b64 v[128:129], %[ga] offset:128\n"
        "v_mfma_f32_16x16x32_bf16 %[a5], v[192:195], v[244:247], %[a5]\n"
        "ds_read_b64 v[130:131], %[gb] offset:128\n"
        "v_mfma_f32_16x16x32_bf16 %[a6], v[192:195], v[248:251], %[a6]\n"
        "ds_read_b128 v[176:179], %[fa] offset:1664\n"
        "v_mfma_f32_16x16x32_bf16 %[a7], v[192:195], v[252:255], %[a7]\n"
        "ds_read_b128 v[180:183], %[fa] offset:1152\n"
        "v_mfma_f32_16x16x32_bf16 %[a7], v[196:199], v[248:251], %[a7]\n"
        "ds_read_b128 v[184:187], %[fa] offset:640\n"
        "v_mfma_f32_16x16x32_bf16 %[a6], v[196:199], v[244:247], %[a6]\n"
        "ds_read_b128 v[188:191], %[fa] offset:128\n"
        "v_mfma_f32_16x16x32_bf16 %[a5], v[196:199], v[240:243], %[a5]\n"
        "ds_read_b64 v[132:133], %[ga] offset:640\n"
        "v_mfma_f32_16x16x32_bf16 %[a4], v[196:199], v[236:239], %[a4]\n"
        "ds_read_b64 v[134:135], %[gb] offset:640\n"
        "v_mfma_f32_16x16x32_bf16 %[a7], v[200:203], v[244:247], %[a7]\n"
        "ds_read_b128 v[172:175], %[fa] offset:2176\n"
        "v_mfma_f32_16x16x32_bf16 %[a6], v[200:203], v[240:243], %[a6]\n"
        "ds_read_b64 v[136:137], %[ga] offset:1152\n"
        "v_mfma_f32_16x16x32_bf16 %[a5], v[200:203], v[236:239], %[a5]\n"
        "ds_read_b64 v[138:139], %[gb] offset:1152\n"
        "v_mfma_f32_16x16x32_bf16 %[a4], v[200:203], v[232:235], %[a4]\n"
        "ds_read_b128 v[168:171], %[fa] offset:2688\n"
        "v_mfma_f32_16x16x32_bf16 %[a7], v[204:207], v[240:243], %[a7]\n"
        "ds_read_b64 v[140:141], %[ga] offset:1664\n"
        "v_mfma_f32_16x16x32_bf16 %[a6], v[204:207], v[236:239], %[a6]\n"
        "ds_read_b64 v[142:143], %[gb] offset:1664\n"
        "v_mfma_f32_16x16x32_bf16 %[a5], v[204:207], v[232:235], %[a5]\n"
        "ds_read_b128 v[164:167], %[fa] offset:3200\n"
        "v_mfma_f32_16x16x32_bf16 %[a4], v[204:207], v[228:231], %[a4]\n"
        "ds_read_b64 v[144:145], %[ga] offset:2176\n"
        "v_mfma_f32_16x16x32_bf16 %[a7], v[208:211], v[236:239], %[a7]\n"
        "ds_read_b64 v[146:147], %[gb] offset:2176\n"
        "v_mfma_f32_16x16x32_bf16 %[a6], v[208:211], v[232:235], %[a6]\n"
        "ds_read_b128 v[160:163], %[fa] offset:3712\n"
        "v_mfma_f32_16x16x32_bf16 %[a5], v[208:211], v[228:231], %[a5]\n"
        "ds_read_b64 v[148:149], %[ga] offset:2688\n"
        "s_waitcnt lgkmcnt(15)\n"
        "v_mfma_f32_16x16x32_bf16 %[a4], v[208:211], v[224:227], %[a4]\n"
        "ds_read_b64 v[150:151], %[gb] offset:2688\n"
        "v_mfma_f32_16x16x32_bf16 %[a5], v[212:215], v[224:227], %[a5]\n"
        "ds_read_b64 v[152:153], %[ga] offset:3200\n"
        "v_mfma_f32_16x16x32_bf16 %[a6], v[212:215], v[228:231], %[a6]\n"
        "ds_read_b64 v[154:155], %[gb] offset:3200\n"
        "v_mfma_f32_16x16x32_bf16 %[a7], v[212:215], v[232:235], %[a7]\n"
        "ds_read_b64 v[156:157], %[ga] offset:3712\n"
        "v_mfma_f32_16x16x32_bf16 %[a6], v[216:219], v[224:227], %[a6]\n"
        "ds_read_b64 v[158:159], %[gb] offset:3712\n"
        "v_mfma_f32_16x16x32_bf16 %[a7], v[216:219], v[228:231], %[a7]\n"
        "v_mfma_f32_16x16x32_bf16 %[a7], v[220:223], v[224:227], %[a7]\n"
        "v_mfma_f32_16x16x32_bf16 %[a4], v[128:131], v[176:179], %[a4]\n"
        "ds_read_b64 v[192:193], %[ga] offset:192\n"
        "v_mfma_f32_16x16x32_bf16 %[a5], v[128:131], v[180:183], %[a5]\n"
        "ds_read_b64 v[194:195], %[gb] offset:192\n"
        "s_waitcnt lgkmcnt(15)\n"
        "v_mfma_f32_16x16x32_bf16 %[a6], v[128:131], v[184:187], %[a6]\n"
        "ds_read_b128 v[240:243], %[fa] offset:1728\n"
        "v_mfma_f32_16x16x32_bf16 %[a7], v[128:131], v[188:191], %[a7]\n"
        "ds_read_b128 v[244:247], %[fa] offset:1216\n"
        "v_mfma_f32_16x16x32_bf16 %[a7], v[132:135], v[184:187], %[a7]\n"
        "ds_read_b128 v[248:251], %[fa] offset:704\n"
        "v_mfma_f32_16x16x32_bf16 %[a6], v[132:135], v[180:183], %[a6]\n"
        "ds_read_b128 v[252:255], %[fa] offset:192\n"
        "v_mfma_f32_16x16x32_bf16 %[a5], v[132:135], v[176:179], %[a5]\n"
        "ds_read_b64 v[196:197], %[ga] offset:704\n"
        "v_mfma_f32_16x16x32_bf16 %[a4], v[132:135], v[172:175], %[a4]\n"
        "ds_read_b64 v[198:199], %[gb] offset:704\n"
        "v_mfma_f32_16x16x32_bf16 %[a7], v[136:139], v[180:183], %[a7]\n"
        "ds_read_b128 v[236:239], %[fa] offset:2240\n"
        "v_mfma_f32_16x16x32_bf16 %[a6], v[136:139], v[176:179], %[a6]\n"
        "ds_read_b64 v[200:201], %[ga] offset:1216\n"
        "v_mfma_f32_16x16x32_bf16 %[a5], v[136:139], v[172:175], %[a5]\n"
        "ds_read_b64 v[202:203], %[gb] offset:1216\n"
        "s_waitcnt lgkmcnt(15)\n"
        "v_mfma_f32_16x16x32_bf16 %[a4], v[136:139], v[168:171], %[a4]\n"
        "ds_read_b128 v[232:235], %[fa] offset:2752\n"
        "v_mfma_f32_16x16x32_bf16 %[a7], v[140:143], v[176:179], %[a7]\n"
        "ds_read_b64 v[204:205], %[ga] offset:1728\n"
        "v_mfma_f32_16x16x32_bf16 %[a6], v[140:143], v[172:175], %[a6]\n"
        "ds_read_b64 v[206:207], %[gb] offset:1728\n"
        "v_mfma_f32_16x16x32_bf16 %[a5], v[140:143], v[168:171], %[a5]\n"
        "ds_read_b128 v[228:231], %[fa] offset:3264\n"
        "v_mfma_f32_16x16x32_bf16 %[a4], v[140:143], v[164:167], %[a4]\n"
        "ds_read_b64 v[208:209], %[ga] offset:2240\n"
        "v_mfma_f32_16x16x32_bf16 %[a7], v[144:147], v[172:175], %[a7]\n"
        "ds_read_b64 v[210:211], %[gb] offset:2240\n"
        "v_mfma_f32_16x16x32_bf16 %[a6], v[144:147], v[168:171], %[a6]\n"
        "ds_read_b128 v[224:227], %[fa] offset:3776\n"
        "v_mfma_f32_16x16x32_bf16 %[a5], v[144:147], v[164:167], %[a5]\n"
        "ds_read_b64 v[212:213], %[ga] offset:2752\n"
        "v_mfma_f32_16x16x32_bf16 %[a4], v[144:147], v[160:163], %[a4]\n"
        "ds_read_b64 v[214:215], %[gb] offset:2752\n"
        "v_mfma_f32_16x16x32_bf16 %[a5], v[148:151], v[160:163], %[a5]\n"
        "ds_read_b64 v[216:217], %[ga] offset:3264\n"
        "v_mfma_f32_16x16x32_bf16 %[a6], v[148:151], v[164:167], %[a6]\n"
        "ds_read_b64 v[218:219], %[gb] offset:3264\n"
        "v_mfma_f32_16x16x32_bf16 %[a7], v[148:151], v[168:171], %[a7]\n"
        "ds_read_b64 v[220:221], %[ga] offset:3776\n"
        "s_waitcnt lgkmcnt(15)\n"
        "v_mfma_f32_16x16x32_bf16 %[a6], v[152:155], v[160:163], %[a6]\n"
        "ds_read_b64 v[222:223], %[gb] offset:3776\n"
        "v_mfma_f32_16x16x32_bf16 %[a7], v[152:155], v[164:167], %[a7]\n"
        "v_mfma_f32_16x16x32_bf16 %[a7], v[156:159], v[160:163], %[a7]\n"
        "v_add_u32 %[ga], 128, %[ga]\n"
        "v_add_u32 %[gb], 128, %[gb]\n"
        "v_add_u32 %[fa], 128, %[fa]\n"
        "s_waitcnt lgkmcnt(8)\n"
        "s_sub_u32 %[cnt], %[cnt], 1\n"
        "s_cmp_lg_u32 %[cnt], 0\n"
        "s_cbranch_scc1 1b\n"
        "v_mfma_f32_16x16x32_bf16 %[a4], v[192:195], v[240:243], %[a4]\n"
        "v_mfma_f32_16x16x32_bf16 %[a5], v[192:195], v[244:247], %[a5]\n"
        "v_mfma_f32_16x16x32_bf16 %[a6], v[192:195], v[248:251], %[a6]\n"
        "v_mfma_f32_16x16x32_bf16 %[a7], v[192:195], v[252:255], %[a7]\n"
        "v_mfma_f32_16x16x32_bf16 %[a7], v[196:199], v[248:251], %[a7]\n"
        "v_mfma_f32_16x16x32_bf16 %[a6], v[196:199], v[244:247], %[a6]\n"
        "v_mfma_f32_16x16x32_bf16 %[a5], v[196:199], v[240:243], %[a5]\n"
        "v_mfma_f32_16x16x32_bf16 %[a4], v[196:199], v[236:239], %[a4]\n"
        "v_mfma_f32_16x16x32_bf16 %[a7], v[200:203], v[244:247], %[a7]\n"
        "v_mfma_f32_16x16x32_bf16 %[a6], v[200:203], v[240:243], %[a6]\n"
        "v_mfma_f32_16x16x32_bf16 %[a5], v[200:203], v[236:239], %[a5]\n"
        "v_mfma_f32_16x16x32_bf16 %[a4], v[200:203], v[232:235], %[a4]\n"
        "v_mfma_f32_16x16x32_bf16 %[a7], v[204:207], v[240:243], %[a7]\n"
        "v_mfma_f32_16x16x32_bf16 %[a6], v[204:207], v[236:239], %[a6]\n"
        "v_mfma_f32_16x16x32_bf16 %[a5], v[204:207], v[232:235], %[a5]\n"
        "v_mfma_f32_16x16x32_bf16 %[a4], v[204:207], v[228:231], %[a4]\n"
        "s_waitcnt lgkmcnt(7)\n"
        "v_mfma_f32_16x16x32_bf16 %[a7], v[208:211], v[236:239], %[a7]\n"
        "v_mfma_f32_16x16x32_bf16 %[a6], v[208:211], v[232:235], %[a6]\n"
        "v_mfma_f32_16x16x32_bf16 %[a5], v[208:211], v[228:231], %[a5]\n"
        "s_waitcnt lgkmcnt(6)\n"
        "v_mfma_f32_16x16x32_bf16 %[a4], v[208:211], v[224:227], %[a4]\n"
        "s_waitcnt lgkmcnt(4)\n"
        "v_mfma_f32_16x16x32_bf16 %[a5], v[212:215], v[224:227], %[a5]\n"
        "v_mfma_f32_16x16x32_bf16 %[a6], v[212:215], v[228:231], %[a6]\n"
        "v_mfma_f32_16x16x32_bf16 %[a7], v[212:215], v[232:235], %[a7]\n"
        "s_waitcnt lgkmcnt(2)\n"
        "v_mfma_f32_16x16x32_bf16 %[a6], v[216:219], v[224:227], %[a6]\n"
        "v_mfma_f32_16x16x32_bf16 %[a7], v[216:219], v[228:231], %[a7]\n"
        "s_waitcnt lgkmcnt(0)\n"
        "v_mfma_f32_16x16x32_bf16 %[a7], v[220:223], v[224:227], %[a7]\n"
        "s_setprio 0\n"
        "s_nop 7\n"
        "s_nop 1\n"
        : [ga] "+v"(ga), [gb] "+v"(gb), [fa] "+v"(fa), [a4] "=&v"(a4), [a5] "=&v"(a5), [a6] "=&v"(a6), [a7] "=&v"(a7), [cnt] "=&s"(cnt)
        : [ya] "v"(ya), [yf] "v"(yf), [one] "v"(1)
        : "memory", "scc", "v104", "v105", "v106", "v107", "v108", "v109", "v110", "v111", "v112", "v113", "v114", "v115", "v116", "v117", "v118", "v119", "v120", "v121", "v122", "v123", "v124", "v125", "v126", "v127", "v128", "v129", "v130", "v131", "v132", "v133", "v134", "v135", "v136", "v137", "v138", "v139", "v140", "v141", "v142", "v143", "v144", "v145", "v146", "v147", "v148", "v149", "v150", "v151", "v152", "v153", "v154", "v155", "v156", "v157", "v158", "v159", "v160", "v161", "v162", "v163", "v164", "v165", "v166", "v167", "v168", "v169", "v170", "v171", "v172", "v173", "v174", "v175", "v176", "v177", "v178", "v179", "v180", "v181", "v182", "v183", "v184", "v185", "v186", "v187", "v188", "v189", "v190", "v191", "v192", "v193", "v194", "v195", "v196", "v197", "v198", "v199", "v200", "v201", "v202", "v203", "v204", "v205", "v206", "v207", "v208", "v209", "v210", "v211", "v212", "v213", "v214", "v215", "v216", "v217", "v218", "v219", "v220", "v221", "v222", "v223", "v224", "v225", "v226", "v227", "v228", "v229", "v230", "v231", "v232", "v233", "v234", "v235", "v236", "v237", "v238", "v239", "v240", "v241", "v242", "v243", "v244", "v245", "v246", "v247", "v248", "v249", "v250", "v251", "v252", "v253", "v254", "v255");
}

__device__ __forceinline__ float dot2(u32 a, u32 b, float c) {
    return __builtin_amdgcn_fdot2_f32_bf16(__builtin_bit_cast(bf16x2, a), __builtin_bit_cast(bf16x2, b), c, false);
}
__device__ __forceinline__ void lds_wait4(int addr, u32 want) {
#pragma unroll 1
    for (u32 spins = 0; spins < (1u << 22); ++spins) {
        const u32x4 f = *(const volatile LAS u32x4*)(size_t)(u32)addr;
        if (__builtin_amdgcn_readfirstlane(f[0] & f[1] & f[2] & f[3]) >= want) break;
        __builtin_amdgcn_s_sleep(3);
    }
    asm volatile("" ::: "memory");
}

constexpr int C_T = L_MISC, C_PUB = L_MISC + 4, C_TB = L_MISC + 8, C_KB = L_MISC + 12, C_KT = L_MISC + 16, C_GO = L_MISC + 20, C_GO2 = L_MISC + 24, C_Y = L_MISC + 32;

__global__ void __launch_bounds__(NT, 2) fused_kernel(Params P) {
    extern __shared__ __attribute__((aligned(16))) char smem_[];
    const int tid = threadIdx.x, lane = tid & 63;
    const int wv = __builtin_amdgcn_readfirstlane(tid >> 6);
    const int w = blockIdx.x, grp = w & 7, ti = w >> 3;
    const int d0 = 64 * grp + 2 * ti;
    char* ws = P.ws;
    const u64 tstart_ = __builtin_amdgcn_s_memrealtime();
    STAMP_DECL
    stamp(ws, 0);
    u32 pfv;
    {
        u64 pc; u32 len;
        asm volatile("s_getpc_b64 %0\n.Lfk_pf_start:\n\ts_mov_b32 %1, .Lfk_pf_end-.Lfk_pf_start" : "=s"(pc), "=s"(len));
        u32 off = (u32)tid * 64u;
        off = off < len - 4u ? off : len - 4u;
        pfv = *(const GAS u32*)(pc + (off & ~3u));
    }
    const __amdgpu_buffer_rsrc_t sig_rsrc = __builtin_amdgcn_make_buffer_rsrc(ws + WS_SIG, 0, 2 * D * S * 2, 0x00020000);
    const __amdgpu_buffer_rsrc_t g_rsrc = __builtin_amdgcn_make_buffer_rsrc(ws + WS_G, 0, S * 4, 0x00020000);

    u32x4 lk[2], lv[2], lq[2], gq[2], lh, gwv, sm[2], gt0;
    u32x2 skw[2], sm2[2], sme, gt1, xt0, xt1;
    u32 pl, pa, pb, pc, pd, pe, pf;
    u64 tag;
    const bool has_halo = (wv == 0) && (ti > 0);
    const int kw = wv - 4, kt = tid - 256, kch = kw >> 1;
    if (wv < 4) {
        const size_t toff = (size_t)(64 * ti + 16 * wv + (lane >> 3)) * D + 64 * grp + 8 * (lane & 7);
#pragma unroll
        for (int u = 0; u < 2; ++u) { lk[u] = *(const u32x4*)(P.k + toff + 8 * u * D); lv[u] = *(const u32x4*)(P.v + toff + 8 * u * D); lq[u] = *(const u32x4*)(P.q + toff + 8 * u * D); }
        lh = *(const u32x4*)(P.k + (has_halo ? toff - 8 * D : toff));
#pragma unroll
        for (int u = 0; u < 2; ++u) {
            skw[u] = *(const u32x2*)(P.sk + (64 * grp + 16 * wv + 8 * u + (lane >> 3)) * 4);
            gq[u] = *(const u32x4*)(P.q + (size_t)(8 * w + 2 * wv + u) * D + 8 * lane);
        }
        gwv = *(const u32x4*)(P.gw + 8 * lane);
        tag = *(const GAS u64*)(ws + WS_EPOCH);
        if (tid < 3) lds_st<u32x4>(L_MISC + 16 * tid, (u32x4){0, 0, 0, 0});
        if (tid >= 64 && tid < 64 + 4 * 30) lds_st<u32x4>(L_SIG + ((tid - 64) / 30) * SIG_ST + 4096 + ((tid - 64) % 30) * 16, (u32x4){0, 0, 0, 0});
    } else {
        const int pidx = (d0 + kch) * NS + 64 * (kw & 1) + lane;
        pl = P.ldt[d0 + kch];
        pa = P.are[pidx]; pb = P.aim[pidx]; pc = P.bre[pidx]; pd = P.bim[pidx]; pe = P.cre[pidx]; pf = P.cim[pidx];
#pragma unroll
        for (int ch = 0; ch < 2; ++ch) {
            const u16* ssrc = P.ssm + (size_t)(d0 + ch) * S + 8 * kt;
            sm[ch] = *(const u32x4*)ssrc;
            sm2[ch] = *(const u32x2*)(ssrc + (kt < 255 ? 8 : 0));
        }
        sme = *(const u32x2*)(P.ssm + (size_t)(d0 + ((lane >> 2) & 1)) * S);
        gt0 = *(const u32x4*)&idx_tabs.g[kt][0];
        gt1 = *(const u32x2*)&idx_tabs.g[kt][8];
        xt0 = *(const u32x2*)&idx_tabs.x[kw % 3][lane][0]; xt1 = *(const u32x2*)&idx_tabs.x[(kw + 1) % 3][lane][0];
        tag = *(const GAS u64*)(ws + WS_EPOCH);
    }
    __builtin_amdgcn_sched_barrier(0);
    __syncthreads();
    __builtin_amdgcn_sched_barrier(0);

    if (wv < 4) {
        __builtin_amdgcn_s_setprio(2);
        asm volatile("" :: "v"(lk[0]), "v"(lk[1]), "v"(lv[0]), "v"(lv[1]), "v"(lq[0]), "v"(lq[1]));
        while (__builtin_amdgcn_s_memrealtime() - tstart_ < (u64)(DELAY_US * 100)) __builtin_amdgcn_s_sleep(2);
        {
            const int r = lane >> 3, o = lane & 7;
            const u32 sel = (r & 1) ? 0x07060302u : 0x01000504u;
            const int c = 8 * o + r;
#pragma unroll
            for (int u = 0; u < 2; ++u) {
                const int a = L_T + c * 128 + (((7 - 2 * wv - u) ^ o) << 4);
                u32x4 t3[3] = {lk[u], lv[u], lq[u]};
                tr8n<3>(t3, sel);
                lds_st<u32x4>(a + 8192, t3[0]);
                lds_st<u32x4>(a + 16384, t3[1]);
                lds_st<u32x4>(a, t3[2]);
            }
            if (wv == 0) { u32x4 h1[1] = {lh}; tr8n<1>(h1, sel); if (!has_halo) h1[0] = (u32x4){0, 0, 0, 0}; lds_st<u32x4>(L_H + c * 16, h1[0]); }
        }
        lds_arrive(C_T, lane);
        stamp(ws, 1);
        lds_await(C_T, 4);
#pragma unroll
        for (int u = 0; u < 2; ++u) {
            const int v8 = 2 * wv + u, oct = lane & 7, c = 8 * v8 + (lane >> 3);
            const int a = L_T + c * 128 + ((oct ^ v8) << 4);
            const u32x4 tk = lds_ld<u32x4>(a + 8192);
            const int an = oct < 7 ? (L_T + 8192 + c * 128 + (((oct + 1) ^ v8) << 4)) : (L_H + c * 16);
            const u32x2 tn = lds_ld<u32x2>(an);
            const u32x4 tv = lds_ld<u32x4>(a + 16384), tq = lds_ld<u32x4>(a);
            const u32 go = ((64 * grp + c) * S + 1984 - 64 * ti + 8 * oct) * 2;
            __builtin_amdgcn_raw_buffer_store_b128(tq, sig_rsrc, go + D * S * 2, 0, 16);
            const u32 dd[6] = {tk[0], tk[1], tk[2], tk[3], tn[0], tn[1]};
            float kv[8];
#pragma unroll
            for (int m = 0; m < 4; ++m) {
                const u32 o0 = __builtin_amdgcn_alignbit(dd[m + 1], dd[m], 16), o1 = __builtin_amdgcn_alignbit(dd[m + 2], dd[m + 1], 16);
                kv[2 * m] = dot2(dd[m], skw[u][0], dot2(dd[m + 1], skw[u][1], 0.f)) * lo2f(tv[m]);
                kv[2 * m + 1] = dot2(o0, skw[u][0], dot2(o1, skw[u][1], 0.f)) * hi2f(tv[m]);
            }
            const u32x4 okv = {pk(kv[0], kv[1]), pk(kv[2], kv[3]), pk(kv[4], kv[5]), pk(kv[6], kv[7])};
            __builtin_amdgcn_raw_buffer_store_b128(okv, sig_rsrc, go, 0, 16);
        }
        stamp(ws, 2);
        {
            float z[2];
#pragma unroll
            for (int u = 0; u < 2; ++u) {
                z[u] = dot2(gq[u][0], gwv[0], 0.f) + dot2(gq[u][1], gwv[1], 0.f);
                z[u] += dot2(gq[u][2], gwv[2], 0.f) + dot2(gq[u][3], gwv[3], 0.f);
            }
            auto h = __builtin_amdgcn_permlane32_swap(__float_as_uint(z[0]), __float_as_uint(z[1]), false, false);
            float y = __uint_as_float(h[0]) + __uint_as_float(h[1]);
            y += __uint_as_float(__builtin_amdgcn_update_dpp(0u, __float_as_uint(y), 0x128, 0xf, 0xf, true));
            y += __uint_as_float(__builtin_amdgcn_update_dpp(0u, __float_as_uint(y), 0x124, 0xf, 0xf, true));
            y += __uint_as_float(__builtin_amdgcn_update_dpp(0u, __float_as_uint(y), 0x122, 0xf, 0xf, true));
            y += __uint_as_float(__builtin_amdgcn_update_dpp(0u, __float_as_uint(y), 0x121, 0xf, 0xf, true));
            { auto s = __builtin_amdgcn_permlane16_swap(__float_as_uint(y), __float_as_uint(y), false, false); y = __uint_as_float(s[0]) + __uint_as_float(s[1]); }
            y = rb(y);
            const float g = __builtin_amdgcn_rcpf(1.f + __builtin_amdgcn_exp2f(y * -1.44269504088896341f));
            if ((lane & 31) == 0) __builtin_amdgcn_raw_buffer_store_b32(pk(g, 0.f), g_rsrc, 4 * (8 * w + 2 * wv + (lane >> 5)), 0, 16);
        }
        {
            asm volatile("s_waitcnt vmcnt(0)" : "+v"(tag) : "v"(pfv) : "memory");
            tag += 1;
            u32 old = 0;
            if (lane == 0) old = __hip_atomic_fetch_add((LAS u32*)(size_t)C_PUB, 1u, __ATOMIC_RELAXED, __HIP_MEMORY_SCOPE_WORKGROUP);
            old = __builtin_amdgcn_readfirstlane(old);
            if (old == 3 && lane == 0) __hip_atomic_store((gu64*)(ws + WS_FLAG1) + (grp * 32 + ti) * FSTR, tag, __ATOMIC_RELAXED, __HIP_MEMORY_SCOPE_AGENT);
        }
        __builtin_amdgcn_s_setprio(0);
        stamp(ws, 4);
        if (wv == 0) {
            __builtin_amdgcn_s_sleep(POLL_DELAY);
            wait_flags((const gu64*)(ws + WS_FLAG1) + (grp * 32 + (lane & 31)) * FSTR, tag);
            lds_arrive(C_GO, lane);
        } else {
            lds_await(C_GO, 1);
            if (wv == 1) {
                const gu64* f = (const gu64*)(ws + WS_FLAG1) + lane * FSTR;
#pragma unroll 1
                for (u32 spins = 0; spins < (1u << 18); ++spins) {
                    const u64 x0 = __hip_atomic_load(f, __ATOMIC_RELAXED, __HIP_MEMORY_SCOPE_AGENT), x1 = __hip_atomic_load(f + 64 * FSTR, __ATOMIC_RELAXED, __HIP_MEMORY_SCOPE_AGENT);
                    const u64 x2 = __hip_atomic_load(f + 128 * FSTR, __ATOMIC_RELAXED, __HIP_MEMORY_SCOPE_AGENT), x3 = __hip_atomic_load(f + 192 * FSTR, __ATOMIC_RELAXED, __HIP_MEMORY_SCOPE_AGENT);
                    if (__all(x0 == tag && x1 == tag && x2 == tag && x3 == tag)) break;
                    __builtin_amdgcn_s_sleep(8);
                }
                lds_arrive(C_GO2, lane);
                if (w == 0 && lane == 0) __hip_atomic_store((gu64*)(ws + WS_EPOCH), tag, __ATOMIC_RELAXED, __HIP_MEMORY_SCOPE_AGENT);
            }
        }
        stamp(ws, 7);
        {
            const u32 go = (((wv & 1) * D + d0 + (wv >> 1)) * S) * 2 + lane * 16;
            const u32x4 v0 = __builtin_amdgcn_raw_buffer_load_b128(sig_rsrc, go, 0, 16), v1 = __builtin_amdgcn_raw_buffer_load_b128(sig_rsrc, go + 1024, 0, 16);
            const u32x4 v2 = __builtin_amdgcn_raw_buffer_load_b128(sig_rsrc, go + 2048, 0, 16), v3 = __builtin_amdgcn_raw_buffer_load_b128(sig_rsrc, go + 3072, 0, 16);
            const int la = L_SIG + wv * SIG_ST + lane * 16;
            lds_st<u32x4>(la, v0); lds_st<u32x4>(la + 1024, v1); lds_st<u32x4>(la + 2048, v2); lds_st<u32x4>(la + 3072, v3);
        }
        stamp(ws, 14);
        lds_await(C_KT, 4);
        stamp(ws, 8);
        const int r = lane & 15, q = lane >> 4;
        const int a = (r + 1) & 3;
        const int ga = ((wv & 1) ? L_X : L_KTA) + (wv >> 1) * KT_CC + a * KT_COPY + 2 * (1 + r + 8 * q - a);
        const int fa = L_SIG + wv * SIG_ST + 2 * (240 - 16 * r + 8 * q);
        const int ya = L_Y + wv * 4096 + 2 * (16 * r + 4 * q);
        f32x4 acc[4];
        conv_all(ga, ga + 8, fa, ya, C_Y + 4 * wv, acc[0], acc[1], acc[2], acc[3]);
        stamp(ws, 9);
#pragma unroll
        for (int ct = 0; ct < 4; ++ct) lds_st<u32x2>(ya + 512 * (4 + ct), (u32x2){pk(acc[ct][0], acc[ct][1]), pk(acc[ct][2], acc[ct][3])});
        __syncthreads();
    } else {
        asm volatile("" : "+v"(pl), "+v"(pa), "+v"(pb), "+v"(pc), "+v"(pd), "+v"(pe), "+v"(pf));
        stamp(ws, 16);
        {
            const float dt = rb(__builtin_amdgcn_exp2f(lo2f(pl) * 1.44269504088896341f));
            const float la = dt * lo2f(pa) * 1.44269504088896341f;
            float rv = dt * lo2f(pb) * 0.15915494309189535f;
            rv -= rintf(rv);
            const u32 tb = L_X + kch * TB_CH + 256 * (kw & 1);
            const cpx w128 = cexp_rev(la * 128.f, rv * 128.f), w1 = cexp_rev(la, rv), u1 = cexp_rev(la * 8.f, rv * -8.f);
            const cpx R0 = cexp_rev(la * 1024.f, __builtin_amdgcn_fractf(rv * 1024.f));
            const float br = dt * lo2f(pc), bi = dt * lo2f(pd), cr = lo2f(pe), ci = lo2f(pf);
            const cpx w256 = cmul(w128, w128), w2 = cmul(w1, w1), u2 = cmul(u1, u1);
            cpx Q[8];
            Q[0] = (cpx){cr * br - ci * bi, -(cr * bi + ci * br)};
            const cpx w3 = cmul(w2, w1), w4 = cmul(w2, w2), u4 = cmul(u2, u2), w384 = cmul(w256, w128), w512 = cmul(w256, w256);
            Q[1] = cmul(Q[0], u1); Q[2] = cmul(Q[0], u2); Q[3] = cmul(Q[1], u2);
            const cpx w5 = cmul(w4, w1), w6 = cmul(w4, w2), w7 = cmul(w4, w3), u8 = cmul(u4, u4);
            st4_addtid<48 * TB_ROW, 49 * TB_ROW, 50 * TB_ROW, 51 * TB_ROW>(tb, pkc(Q[0]), pkc(Q[1]), pkc(Q[2]), pkc(Q[3]));
            st4_addtid<0 * TB_ROW, 1 * TB_ROW, 2 * TB_ROW, 3 * TB_ROW>(tb, 0x3f80u, pkc(w1), pkc(w2), pkc(w3));
#pragma unroll
            for (int b = 0; b < 4; ++b) Q[4 + b] = cmul(Q[b], u4);
            st4_addtid<4 * TB_ROW, 5 * TB_ROW, 6 * TB_ROW, 7 * TB_ROW>(tb, pkc(w4), pkc(w5), pkc(w6), pkc(w7));
            st4_addtid<52 * TB_ROW, 53 * TB_ROW, 54 * TB_ROW, 55 * TB_ROW>(tb, pkc(Q[4]), pkc(Q[5]), pkc(Q[6]), pkc(Q[7]));
            st4_addtid<8 * TB_ROW, 9 * TB_ROW, 10 * TB_ROW, 11 * TB_ROW>(tb, pkc(w128), pkc(cmul(w128, w1)), pkc(cmul(w128, w2)), pkc(cmul(w128, w3)));
            st4_addtid<12 * TB_ROW, 13 * TB_ROW, 14 * TB_ROW, 15 * TB_ROW>(tb, pkc(cmul(w128, w4)), pkc(cmul(w128, w5)), pkc(cmul(w128, w6)), pkc(cmul(w128, w7)));
            st4_addtid<56 * TB_ROW, 57 * TB_ROW, 58 * TB_ROW, 59 * TB_ROW>(tb, pkc(cmul(Q[0], u8)), pkc(cmul(Q[1], u8)), pkc(cmul(Q[2], u8)), pkc(cmul(Q[3], u8)));
            st4_addtid<60 * TB_ROW, 61 * TB_ROW, 62 * TB_ROW, 63 * TB_ROW>(tb, pkc(cmul(Q[4], u8)), pkc(cmul(Q[5], u8)), pkc(cmul(Q[6], u8)), pkc(cmul(Q[7], u8)));
            st4_addtid<16 * TB_ROW, 17 * TB_ROW, 18 * TB_ROW, 19 * TB_ROW>(tb, pkc(w256), pkc(cmul(w256, w2)), pkc(cmul(w256, w4)), pkc(cmul(w256, w6)));
            st4_addtid<20 * TB_ROW, 21 * TB_ROW, 22 * TB_ROW, 23 * TB_ROW>(tb, pkc(w384), pkc(cmul(w384, w2)), pkc(cmul(w384, w4)), pkc(cmul(w384, w6)));
            const cpx z1 = cmul(w512, w128), z2 = cmul(w512, w256), z3 = cmul(w512, w384);
            st4_addtid<24 * TB_ROW, 25 * TB_ROW, 26 * TB_ROW, 27 * TB_ROW>(tb, pkc(w512), pkc(cmul(w512, w4)), pkc(z1), pkc(cmul(z1, w4)));
            st4_addtid<28 * TB_ROW, 29 * TB_ROW, 30 * TB_ROW, 31 * TB_ROW>(tb, pkc(z2), pkc(cmul(z2, w4)), pkc(z3), pkc(cmul(z3, w4)));
            const cpx R1 = cmul(R0, w128), R2 = cmul(R0, w256), R3 = cmul(R1, w256);
            st4_addtid<32 * TB_ROW, 33 * TB_ROW, 34 * TB_ROW, 35 * TB_ROW>(tb, pkc(R0), pkc(R1), pkc(R2), pkc(R3));
            st4_addtid<36 * TB_ROW, 37 * TB_ROW, 38 * TB_ROW, 39 * TB_ROW>(tb, pkc(cmul(R0, w512)), pkc(cmul(R1, w512)), pkc(cmul(R2, w512)), pkc(cmul(R3, w512)));
            lds_st<u32>(tb + 40 * TB_ROW + 4 * lane, pkc(cmul(R0, R0)));
        }
        lds_arrive(C_TB, lane);
        stamp(ws, 3);
#pragma unroll
        for (int ch = 0; ch < 2; ++ch) write_copies(L_KTA + ch * KT_CC, kt, sm[ch][0], sm[ch][1], sm[ch][2], sm[ch][3], sm2[ch][0], kt < 255 ? sm2[ch][1] : 0u);
        if (kw == 0) write_edges(L_KTA, lane, sme[0], sme[1]);
        lds_await(C_TB, 4);
        stamp(ws, 5);
#pragma unroll
        for (int jb = 0; jb < 2; ++jb) {
            const int job = kw + 4 * jb;
            if (job < 6) {
                const int kc = job >= 3, rt = job - 3 * kc, r = lane & 15, q = lane >> 4;
                const int ab = L_X + kc * TB_CH + (16 * rt + r) * TB_ROW + 16 * q, bb = L_X + kc * TB_CH + (48 + r) * TB_ROW + 16 * q;
                u32x4 A[8], B[8];
#pragma unroll
                for (int t = 0; t < 8; ++t) { A[t] = lds_ld<u32x4>(ab + 64 * t); B[t] = lds_ld<u32x4>(bb + 64 * t); }
                asm volatile("" : "+v"(A[0]), "+v"(A[1]), "+v"(A[2]), "+v"(A[3]), "+v"(A[4]), "+v"(A[5]), "+v"(A[6]), "+v"(A[7]),
                                  "+v"(B[0]), "+v"(B[1]), "+v"(B[2]), "+v"(B[3]), "+v"(B[4]), "+v"(B[5]), "+v"(B[6]), "+v"(B[7]));
                f32x4 acc0 = {0, 0, 0, 0}, acc1 = {0, 0, 0, 0};
#pragma unroll
                for (int t = 0; t < 8; t += 2) {
                    acc0 = __builtin_amdgcn_mfma_f32_16x16x32_bf16(__builtin_bit_cast(bf16x8, A[t]), __builtin_bit_cast(bf16x8, B[t]), acc0, 0, 0, 0);
                    acc1 = __builtin_amdgcn_mfma_f32_16x16x32_bf16(__builtin_bit_cast(bf16x8, A[t + 1]), __builtin_bit_cast(bf16x8, B[t + 1]), acc1, 0, 0, 0);
                }
                const u32x2 xt = jb ? xt1 : xt0;
                const int kb = L_KB + kc * 1312;
                lds_st<u16>(kb + (xt[0] & 0xffff), (u16)pk(acc0[0] + acc1[0], 0.f));
                lds_st<u16>(kb + (xt[0] >> 16), (u16)pk(acc0[1] + acc1[1], 0.f));
                lds_st<u16>(kb + (xt[1] & 0xffff), (u16)pk(acc0[2] + acc1[2], 0.f));
                lds_st<u16>(kb + (xt[1] >> 16), (u16)pk(acc0[3] + acc1[3], 0.f));
            }
        }
        lds_arrive(C_KB, lane);
        stamp(ws, 13);
        lds_await(C_KB, 4);
#pragma unroll
        for (int ch = 0; ch < 2; ++ch) {
            const int kb = L_KB + ch * 1312;
            u32 e[11];
#pragma unroll
            for (int m = 0; m < 11; ++m) {
                const u32 t = m < 8 ? gt0[m >> 1] : gt1[(m - 8) >> 1];
                e[m] = lds_ld<u16>(kb + ((m & 1) ? (t >> 16) : (t & 0xffff)));
            }
            write_copies(L_X + ch * KT_CC, kt, e[0] | (e[1] << 16), e[2] | (e[3] << 16), e[4] | (e[5] << 16), e[6] | (e[7] << 16), e[8] | (e[9] << 16), e[10]);
        }
        if (kw == 0) { const u32x2 k03 = lds_ld<u32x2>(L_KB + ((lane >> 2) & 1) * 1312); write_edges(L_X, lane, k03[0], k03[1]); }
        lds_arrive(C_KT, lane);
        stamp(ws, 6);
        lds_await(C_GO2, 1);
        asm volatile("" :: "v"(pfv), "v"(tag));
        stamp(ws, 15);
        const int t4 = 4 * kt;
        u32x4 gg[2]; u32x2 sv[2][4];
#pragma unroll
        for (int h = 0; h < 2; ++h) {
            gg[h] = __builtin_amdgcn_raw_buffer_load_b128(g_rsrc, 4 * t4 + 4096 * h, 0, 16);
#pragma unroll
            for (int sg = 0; sg < 4; ++sg)
                sv[h][sg] = __builtin_bit_cast(u32x2, __builtin_amdgcn_raw_buffer_load_b64(sig_rsrc, (((sg & 1) * D + d0 + (sg >> 1)) * S + 2044 - t4 - 1024 * h) * 2, 0, 16));
        }
        const float sD0 = bf2f(P.sD[d0]), sD1 = bf2f(P.sD[d0 + 1]), dk0 = bf2f(P.dsk[d0]), dk1 = bf2f(P.dsk[d0 + 1]);
        float cA[2][2][4], cB[2][4], c0[2][2][4];
#pragma unroll
        for (int h = 0; h < 2; ++h) {
#pragma unroll
            for (int i = 0; i < 4; ++i) {
                const int ri = 3 - i;
                const float g = lo2f(gg[h][i]), omg = rb(1.f - g);
                cB[h][i] = omg;
#pragma unroll
                for (int ch = 0; ch < 2; ++ch) {
                    const u32x2 kv = sv[h][2 * ch], x = sv[h][2 * ch + 1];
                    const float fkv = (ri & 1) ? hi2f(kv[ri >> 1]) : lo2f(kv[ri >> 1]), fx = (ri & 1) ? hi2f(x[ri >> 1]) : lo2f(x[ri >> 1]);
                    cA[h][ch][i] = fmul(g, fx);
                    c0[h][ch][i] = fmul(fx, ffma(fmul(g, ch ? sD1 : sD0), fkv, fmul(omg, ch ? dk1 : dk0)));
                    asm volatile("" : "+v"(cA[h][ch][i]), "+v"(c0[h][ch][i]));
                }
                asm volatile("" : "+v"(cB[h][i]));
            }
        }
        stamp(ws, 9);
#pragma unroll
        for (int h = 0; h < 2; ++h) {
            if (h == 0) lds_wait4(C_Y, 1); else __syncthreads();
            if (h == 0) stamp(ws, 12);
            const int s0 = t4 + 1024 * h;
            float res[2][4];
#pragma unroll
            for (int ch = 0; ch < 2; ++ch) {
                const u32x2 ya = lds_ld<u32x2>(L_Y + (2 * ch) * 4096 + 2 * s0), yb = lds_ld<u32x2>(L_Y + (2 * ch + 1) * 4096 + 2 * s0);
#pragma unroll
                for (int i = 0; i < 4; ++i) {
                    const float fya = (i & 1) ? hi2f(ya[i >> 1]) : lo2f(ya[i >> 1]), fyb = (i & 1) ? hi2f(yb[i >> 1]) : lo2f(yb[i >> 1]);
                    res[ch][i] = ffma(cA[h][ch][i], fya, ffma(cB[h][i], fyb, c0[h][ch][i]));
                }
            }
#pragma unroll
            for (int i = 0; i < 4; ++i) *(u32*)(P.out + (size_t)(s0 + i) * D + d0) = pk(res[0][i], res[1][i]);
            if (h == 0) stamp(ws, 10);
        }
    }
    stamp(ws, 11);
    STAMP_FLUSH;
    asm volatile(".Lfk_pf_end:");
}

extern "C" void kernel_launch(void* const* d_in, const int* in_sizes, int n_in, void* d_out, int out_size, void* d_ws, size_t ws_size, hipStream_t stream) {
    Params p;
    p.q = (const u16*)d_in[0]; p.k = (const u16*)d_in[1]; p.v = (const u16*)d_in[2]; p.sk = (const u16*)d_in[3];
    p.ssm = (const u16*)d_in[4]; p.sD = (const u16*)d_in[5];
    p.are = (const u16*)d_in[7]; p.aim = (const u16*)d_in[8]; p.bre = (const u16*)d_in[9]; p.bim = (const u16*)d_in[10];
    p.cre = (const u16*)d_in[11]; p.cim = (const u16*)d_in[12]; p.ldt = (const u16*)d_in[13]; p.dsk = (const u16*)d_in[14]; p.gw = (const u16*)d_in[15];
    p.out = (u16*)d_out; p.ws = (char*)d_ws;
    (void)hipFuncSetAttribute((const void*)fused_kernel, hipFuncAttributeMaxDynamicSharedMemorySize, L_END);
    fused_kernel<<<NWG, NT, L_END, stream>>>(p);
}
```

```cpp
#include <hip/hip_runtime.h>
#include <stdint.h>

typedef unsigned short u16;
typedef unsigned int u32;
typedef unsigned long long u64;
typedef u32 u32x4 __attribute__((ext_vector_type(4)));
typedef u32 u32x2 __attribute__((ext_vector_type(2)));
typedef float f32x4 __attribute__((ext_vector_type(4)));
typedef float f32x2 __attribute__((ext_vector_type(2)));
typedef __bf16 bf16x2 __attribute__((ext_vector_type(2)));
typedef __bf16 bf16x8 __attribute__((ext_vector_type(8)));
#define GAS __attribute__((address_space(1)))
#define LAS __attribute__((address_space(3)))
typedef GAS u64 gu64;

constexpr int NFIX = 6;
constexpr int FSTR = 16;

constexpr int S = 2048, D = 512, NS = 128;
constexpr int NWG = 256, NT = 512;

constexpr size_t WS_EPOCH = 0;
constexpr size_t WS_FLAG1 = 4096;
constexpr size_t WS_FLAG2 = 40960;
constexpr size_t WS_G     = 49152;
constexpr size_t WS_SIG   = 65536;

constexpr int KT_COPY = 4160;
constexpr int KT_CC   = 4 * KT_COPY;
constexpr int TB_ROW  = 528, TB_CH = 64 * TB_ROW;
constexpr int L_X     = 0;
constexpr int L_Y     = L_X + 2 * KT_CC;
constexpr int L_KTA   = L_X + 2 * TB_CH;
constexpr int SIG_ST  = 4608;
constexpr int L_SIG   = L_KTA + 2 * KT_CC;
constexpr int L_T     = L_SIG + 4 * SIG_ST;
constexpr int L_H     = L_T + 3 * 8192;
constexpr int L_KB    = L_H + 1024;
constexpr int L_MISC  = L_KB + 2 * 1312;
constexpr int L_END   = L_MISC + 32;

__device__ __forceinline__ u32 pk(float lo, float hi) { f32x2 v = {lo, hi}; bf16x2 b = __builtin_convertvector(v, bf16x2); return __builtin_bit_cast(u32, b); }
__device__ __forceinline__ float rb(float x) { return __uint_as_float(pk(0.f, x)); }
__device__ __forceinline__ float lo2f(u32 d) { return __uint_as_float(d << 16); }
__device__ __forceinline__ float hi2f(u32 d) { return __uint_as_float(d & 0xffff0000u); }

template <typename T> __device__ __forceinline__ T lds_ld(int off) { return *(const LAS T*)(size_t)(u32)off; }
template <typename T> __device__ __forceinline__ void lds_st(int off, T v) { *(LAS T*)(size_t)(u32)off = v; }


struct IdxTabs { u16 g[256][12]; u16 x[3][64][4]; };
constexpr int bf16_round_int(int l) {
    int sh = 0; while ((l >> sh) >= 256) ++sh;
    if (sh == 0) return l;
    const int q = l >> sh, rem = l & ((1 << sh) - 1), half = 1 << (sh - 1);
    const int up = rem > half || (rem == half && (q & 1));
    return (q + up) << sh;
}
constexpr int kt_index(int l) {
    const int v = bf16_round_int(l);
    return v < 256 ? v : v < 512 ? 256 + (v - 256) / 2 : v < 1024 ? 384 + (v - 512) / 4 : 512 + (v - 1024) / 8;
}
constexpr IdxTabs make_tabs() {
    IdxTabs t{};
    for (int j = 0; j < 256; ++j)
        for (int m = 0; m < 12; ++m) { int l = 8 * j + m; if (l > 2047) l = 2047; t.g[j][m] = (u16)(2 * kt_index(l)); }
    for (int rt = 0; rt < 3; ++rt)
        for (int lane = 0; lane < 64; ++lane)
            for (int i = 0; i < 4; ++i) {
                const int row = 4 * (lane >> 4) + i, b = lane & 15;
                int idx = 0;
                if (rt == 0) idx = (row < 8 ? row : 120 + row) + 8 * b;
                else if (rt == 1) idx = row < 8 ? 256 + 64 * (row >> 2) + (row & 3) + 4 * b : 384 + 32 * ((row - 8) >> 1) + (row & 1) + 2 * b;
                else idx = row < 8 ? 512 + 16 * row + b : ((row == 8 && b == 0) ? 640 : 648);
                t.x[rt][lane][i] = (u16)(2 * idx);
            }
    return t;
}
__device__ static const IdxTabs idx_tabs = make_tabs();

struct Params {
    const u16 *q, *k, *v, *sk, *ssm, *sD, *are, *aim, *bre, *bim, *cre, *cim, *ldt, *dsk, *gw;
    u16* out; char* ws;
};

template <int N>
__device__ __forceinline__ void tr8n(u32x4 (&d)[N], u32 sel) {
    u32 e[N][4];
#pragma unroll
    for (int n = 0; n < N; ++n)
#pragma unroll
        for (int m = 0; m < 4; ++m) e[n][m] = __builtin_amdgcn_update_dpp(0u, d[n][m], 0x128, 0xf, 0xf, true);
#pragma unroll
    for (int n = 0; n < N; ++n)
#pragma unroll
        for (int m = 0; m < 4; ++m) e[n][m] = __builtin_amdgcn_perm(e[n][m], d[n][m], sel);
#pragma unroll
    for (int n = 0; n < N; ++n) {
        auto a = __builtin_amdgcn_permlane16_swap(e[n][0], e[n][1], false, false);
        auto b = __builtin_amdgcn_permlane16_swap(e[n][2], e[n][3], false, false);
        e[n][0] = a[0]; e[n][1] = a[1]; e[n][2] = b[0]; e[n][3] = b[1];
    }
#pragma unroll
    for (int n = 0; n < N; ++n) {
        auto c = __builtin_amdgcn_permlane32_swap(e[n][0], e[n][2], false, false);
        auto f = __builtin_amdgcn_permlane32_swap(e[n][1], e[n][3], false, false);
        d[n] = (u32x4){f[1], c[1], f[0], c[0]};
    }
}

__device__ __forceinline__ void write_copies(int base, int j, u32 d0, u32 d1, u32 d2, u32 d3, u32 d4, u32 d5) {
    u32 a10 = __builtin_amdgcn_alignbit(d1, d0, 16), a21 = __builtin_amdgcn_alignbit(d2, d1, 16), a32 = __builtin_amdgcn_alignbit(d3, d2, 16),
        a43 = __builtin_amdgcn_alignbit(d4, d3, 16), a54 = __builtin_amdgcn_alignbit(d5, d4, 16);
    int o = base + 32 + 16 * j;
    lds_st<u32x4>(o, (u32x4){d0, d1, d2, d3});
    lds_st<u32x4>(o + KT_COPY, (u32x4){a10, a21, a32, a43});
    lds_st<u32x4>(o + 2 * KT_COPY, (u32x4){d1, d2, d3, d4});
    lds_st<u32x4>(o + 3 * KT_COPY, (u32x4){a21, a32, a43, a54});
}
__device__ __forceinline__ void write_edges(int base, int lane, u32 k01, u32 k23) {
    const int a = lane & 3, ch = (lane >> 2) & 1, z = lane >> 3;
    u32 d2 = a == 3 ? k01 << 16 : 0u;
    u32 d3 = a == 0 ? 0u : a == 1 ? k01 << 16 : a == 2 ? k01 : __builtin_amdgcn_alignbit(k23, k01, 16);
    if (z) { d2 = 0u; d3 = 0u; }
    if (lane < 16) lds_st<u32x4>(base + ch * KT_CC + a * KT_COPY + (z ? 0 : 16), (u32x4){0u, 0u, d2, d3});
}
__device__ __forceinline__ void lds_arrive(int addr, int lane) {
    asm volatile("" ::: "memory");
    if (lane == 0) __hip_atomic_fetch_add((LAS u32*)(size_t)(u32)addr, 1u, __ATOMIC_RELAXED, __HIP_MEMORY_SCOPE_WORKGROUP);
}
__device__ __forceinline__ void lds_await(int addr, u32 n) {
#pragma unroll 1
    for (u32 spins = 0; spins < (1u << 24); ++spins)
        if (*(const volatile LAS u32*)(size_t)(u32)addr >= n) break;
    asm volatile("" ::: "memory");
}

__device__ __forceinline__ bool wait_flags(const gu64* f, u64 tag) {
#pragma unroll 1
    for (u32 spins = 0; spins < (1u << 18); ++spins) {
        u64 x = __hip_atomic_load(f, __ATOMIC_RELAXED, __HIP_MEMORY_SCOPE_AGENT);
        if (__all(x == tag)) return true;
        __builtin_amdgcn_s_sleep(4);
    }
    return false;
}

typedef f32x2 cpx;
__device__ __forceinline__ cpx cmul(cpx a, cpx b) {
    cpx t, r;
    asm("v_pk_mul_f32 %0, %1, %2 op_sel_hi:[0,1]" : "=v"(t) : "v"(a), "v"(b));
    asm("v_pk_fma_f32 %0, %1, %2, %3 op_sel:[1,1,0] op_sel_hi:[1,0,1] neg_lo:[1,0,0]" : "=v"(r) : "v"(a), "v"(b), "v"(t));
    return r;
}
__device__ __forceinline__ cpx cexp_rev(float lm, float rev) {
    const float e = __builtin_amdgcn_exp2f(lm);
    return (cpx){e * __builtin_amdgcn_cosf(rev), e * __builtin_amdgcn_sinf(rev)};
}
__device__ __forceinline__ u32 pkc(cpx a) { return pk(a.x, a.y); }
__device__ __forceinline__ float ffma(float a, float b, float c) { float r; asm("v_fma_f32 %0, %1, %2, %3" : "=v"(r) : "v"(a), "v"(b), "v"(c)); return r; }
__device__ __forceinline__ float fmul(float a, float b) { float r; asm("v_mul_f32 %0, %1, %2" : "=v"(r) : "v"(a), "v"(b)); return r; }
template <int O0, int O1, int O2, int O3>
__device__ __forceinline__ void st4_addtid(u32 base, u32 a, u32 b, u32 c, u32 d) {
    asm volatile("s_mov_b32 m0, %0\n\ts_nop 0\n\t"
                 "ds_write_addtid_b32 %1 offset:%5\n\tds_write_addtid_b32 %2 offset:%6\n\tds_write_addtid_b32 %3 offset:%7\n\tds_write_addtid_b32 %4 offset:%8"
                 :: "s"(base), "v"(a), "v"(b), "v"(c), "v"(d), "i"(O0), "i"(O1), "i"(O2), "i"(O3) : "memory");
}

__device__ __forceinline__ void conv_all(int ga, int gb, int fa, int ya, f32x4 (&acc)[2]) {
    u32 cnt;
    asm volatile(
        "s_setprio 1\n"
        "ds_read_b64 v[128:129], %[ga] offset:0\n"
        "ds_read_b64 v[130:131], %[gb] offset:0\n"
        "ds_read_b128 v[144:147], %[fa] offset:3584\n"
        "ds_read_b128 v[148:151], %[fa] offset:3072\n"
        "ds_read_b128 v[152:155], %[fa] offset:2560\n"
        "ds_read_b128 v[156:159], %[fa] offset:2048\n"
        "ds_read_b64 v[132:133], %[ga] offset:512\n"
        "ds_read_b64 v[134:135], %[gb] offset:512\n"
        "ds_read_b64 v[136:137], %[ga] offset:1024\n"
        "ds_read_b64 v[138:139], %[gb] offset:1024\n"
        "ds_read_b64 v[140:141], %[ga] offset:1536\n"
        "ds_read_b64 v[142:143], %[gb] offset:1536\n"
        "ds_read_b64 v[160:161], %[ga] offset:64\n"
        "ds_read_b64 v[162:163], %[gb] offset:64\n"
        "ds_read_b128 v[176:179], %[fa] offset:3648\n"
        "ds_read_b128 v[180:183], %[fa] offset:3136\n"
        "ds_read_b128 v[184:187], %[fa] offset:2624\n"
        "ds_read_b128 v[188:191], %[fa] offset:2112\n"
        "ds_read_b64 v[164:165], %[ga] offset:576\n"
        "ds_read_b64 v[166:167], %[gb] offset:576\n"
        "ds_read_b64 v[168:169], %[ga] offset:1088\n"
        "ds_read_b64 v[170:171], %[gb] offset:1088\n"
        "ds_read_b64 v[172:173], %[ga] offset:1600\n"
        "ds_read_b64 v[174:175], %[gb] offset:1600\n"
        "s_waitcnt lgkmcnt(11)\n"
        "v_mfma_f32_16x16x32_bf16 v[96:99], v[128:131], v[144:147], 0\n"
        "ds_read_b64 v[192:193], %[ga] offset:128\n"
        "ds_read_b64 v[194:195], %[gb] offset:128\n"
        "v_mfma_f32_16x16x32_bf16 v[100:103], v[128:131], v[148:151], 0\n"
        "ds_read_b128 v[208:211], %[fa] offset:3712\n"
        "v_mfma_f32_16x16x32_bf16 v[104:107], v[128:131], v[152:155], 0\n"
        "ds_read_b128 v[212:215], %[fa] offset:3200\n"
        "v_mfma_f32_16x16x32_bf16 v[108:111], v[128:131], v[156:159], 0\n"
        "ds_read_b128 v[216:219], %[fa] offset:2688\n"
        "ds_read_b128 v[220:223], %[fa] offset:2176\n"
        "v_mfma_f32_16x16x32_bf16 v[100:103], v[132:135], v[144:147], v[100:103]\n"
        "ds_read_b64 v[196:197], %[ga] offset:640\n"
        "v_mfma_f32_16x16x32_bf16 v[104:107], v[132:135], v[148:151], v[104:107]\n"
        "ds_read_b64 v[198:199], %[gb] offset:640\n"
        "v_mfma_f32_16x16x32_bf16 v[108:111], v[132:135], v[152:155], v[108:111]\n"
        "ds_read_b64 v[200:201], %[ga] offset:1152\n"
        "ds_read_b64 v[202:203], %[gb] offset:1152\n"
        "v_mfma_f32_16x16x32_bf16 v[104:107], v[136:139], v[144:147], v[104:107]\n"
        "ds_read_b64 v[204:205], %[ga] offset:1664\n"
        "v_mfma_f32_16x16x32_bf16 v[108:111], v[136:139], v[148:151], v[108:111]\n"
        "ds_read_b64 v[206:207], %[gb] offset:1664\n"
        "v_mfma_f32_16x16x32_bf16 v[108:111], v[140:143], v[144:147], v[108:111]\n"
        "ds_read_b64 v[224:225], %[ga] offset:192\n"
        "ds_read_b64 v[226:227], %[gb] offset:192\n"
        "s_waitcnt lgkmcnt(11)\n"
        "v_mfma_f32_16x16x32_bf16 v[96:99], v[160:163], v[176:179], v[96:99]\n"
        "ds_read_b128 v[240:243], %[fa] offset:3776\n"
        "v_mfma_f32_16x16x32_bf16 v[100:103], v[160:163], v[180:183], v[100:103]\n"
        "ds_read_b128 v[244:247], %[fa] offset:3264\n"
        "v_mfma_f32_16x16x32_bf16 v[104:107], v[160:163], v[184:187], v[104:107]\n"
        "ds_read_b128 v[248:251], %[fa] offset:2752\n"
        "ds_read_b128 v[252:255], %[fa] offset:2240\n"
        "v_mfma_f32_16x16x32_bf16 v[108:111], v[160:163], v[188:191], v[108:111]\n"
        "ds_read_b64 v[228:229], %[ga] offset:704\n"
        "v_mfma_f32_16x16x32_bf16 v[100:103], v[164:167], v[176:179], v[100:103]\n"
        "ds_read_b64 v[230:231], %[gb] offset:704\n"
        "v_mfma_f32_16x16x32_bf16 v[104:107], v[164:167], v[180:183], v[104:107]\n"
        "ds_read_b64 v[232:233], %[ga] offset:1216\n"
        "ds_read_b64 v[234:235], %[gb] offset:1216\n"
        "v_mfma_f32_16x16x32_bf16 v[108:111], v[164:167], v[184:187], v[108:111]\n"
        "ds_read_b64 v[236:237], %[ga] offset:1728\n"
        "v_mfma_f32_16x16x32_bf16 v[104:107], v[168:171], v[176:179], v[104:107]\n"
        "ds_read_b64 v[238:239], %[gb] offset:1728\n"
        "v_mfma_f32_16x16x32_bf16 v[108:111], v[168:171], v[180:183], v[108:111]\n"
        "v_mfma_f32_16x16x32_bf16 v[108:111], v[172:175], v[176:179], v[108:111]\n"
        "v_mfma_f32_16x16x32_bf16 v[96:99], v[192:195], v[208:211], v[96:99]\n"
        "ds_read_b64 v[128:129], %[ga] offset:256\n"
        "ds_read_b64 v[130:131], %[gb] offset:256\n"
        "s_waitcnt lgkmcnt(11)\n"
        "v_mfma_f32_16x16x32_bf16 v[100:103], v[192:195], v[212:215], v[100:103]\n"
        "ds_read_b128 v[144:147], %[fa] offset:3840\n"
        "v_mfma_f32_16x16x32_bf16 v[104:107], v[192:195], v[216:219], v[104:107]\n"
        "ds_read_b128 v[148:151], %[fa] offset:3328\n"
        "v_mfma_f32_16x16x32_bf16 v[108:111], v[192:195], v[220:223], v[108:111]\n"
        "ds_read_b128 v[152:155], %[fa] offset:2816\n"
        "ds_read_b128 v[156:159], %[fa] offset:2304\n"
        "v_mfma_f32_16x16x32_bf16 v[100:103], v[196:199], v[208:211], v[100:103]\n"
        "ds_read_b64 v[132:133], %[ga] offset:768\n"
        "v_mfma_f32_16x16x32_bf16 v[104:107], v[196:199], v[212:215], v[104:107]\n"
        "ds_read_b64 v[134:135], %[gb] offset:768\n"
        "v_mfma_f32_16x16x32_bf16 v[108:111], v[196:199], v[216:219], v[108:111]\n"
        "ds_read_b64 v[136:137], %[ga] offset:1280\n"
        "ds_read_b64 v[138:139], %[gb] offset:1280\n"
        "v_mfma_f32_16x16x32_bf16 v[104:107], v[200:203], v[208:211], v[104:107]\n"
        "ds_read_b64 v[140:141], %[ga] offset:1792\n"
        "v_mfma_f32_16x16x32_bf16 v[108:111], v[200:203], v[212:215], v[108:111]\n"
        "ds_read_b64 v[142:143], %[gb] offset:1792\n"
        "v_mfma_f32_16x16x32_bf16 v[108:111], v[204:207], v[208:211], v[108:111]\n"
        "ds_read_b64 v[160:161], %[ga] offset:320\n"
        "ds_read_b64 v[162:163], %[gb] offset:320\n"
        "v_mfma_f32_16x16x32_bf16 v[96:99], v[224:227], v[240:243], v[96:99]\n"
        "ds_read_b128 v[176:179], %[fa] offset:3904\n"
        "s_waitcnt lgkmcnt(11)\n"
        "v_mfma_f32_16x16x32_bf16 v[100:103], v[224:227], v[244:247], v[100:103]\n"
        "ds_read_b128 v[180:183], %[fa] offset:3392\n"
        "v_mfma_f32_16x16x32_bf16 v[104:107], v[224:227], v[248:251], v[104:107]\n"
        "ds_read_b128 v[184:187], %[fa] offset:2880\n"
        "ds_read_b128 v[188:191], %[fa] offset:2368\n"
        "v_mfma_f32_16x16x32_bf16 v[108:111], v[224:227], v[252:255], v[108:111]\n"
        "ds_read_b64 v[164:165], %[ga] offset:832\n"
        "v_mfma_f32_16x16x32_bf16 v[100:103], v[228:231], v[240:243], v[100:103]\n"
        "ds_read_b64 v[166:167], %[gb] offset:832\n"
        "v_mfma_f32_16x16x32_bf16 v[104:107], v[228:231], v[244:247], v[104:107]\n"
        "ds_read_b64 v[168:169], %[ga] offset:1344\n"
        "ds_read_b64 v[170:171], %[gb] offset:1344\n"
        "v_mfma_f32_16x16x32_bf16 v[108:111], v[228:231], v[248:251], v[108:111]\n"
        "ds_read_b64 v[172:173], %[ga] offset:1856\n"
        "v_mfma_f32_16x16x32_bf16 v[104:107], v[232:235], v[240:243], v[104:107]\n"
        "ds_read_b64 v[174:175], %[gb] offset:1856\n"
        "v_mfma_f32_16x16x32_bf16 v[108:111], v[232:235], v[244:247], v[108:111]\n"
        "v_mfma_f32_16x16x32_bf16 v[108:111], v[236:239], v[240:243], v[108:111]\n"
        "v_mfma_f32_16x16x32_bf16 v[96:99], v[128:131], v[144:147], v[96:99]\n"
        "ds_read_b64 v[192:193], %[ga] offset:384\n"
        "ds_read_b64 v[194:195], %[gb] offset:384\n"
        "v_mfma_f32_16x16x32_bf16 v[100:103], v[128:131], v[148:151], v[100:103]\n"
        "ds_read_b128 v[208:211], %[fa] offset:3968\n"
        "s_waitcnt lgkmcnt(11)\n"
        "v_mfma_f32_16x16x32_bf16 v[104:107], v[128:131], v[152:155], v[104:107]\n"
        "ds_read_b128 v[212:215], %[fa] offset:3456\n"
        "v_mfma_f32_16x16x32_bf16 v[108:111], v[128:131], v[156:159], v[108:111]\n"
        "ds_read_b128 v[216:219], %[fa] offset:2944\n"
        "ds_read_b128 v[220:223], %[fa] offset:2432\n"
        "v_mfma_f32_16x16x32_bf16 v[100:103], v[132:135], v[144:147], v[100:103]\n"
        "ds_read_b64 v[196:197], %[ga] offset:896\n"
        "v_mfma_f32_16x16x32_bf16 v[104:107], v[132:135], v[148:151], v[104:107]\n"
        "ds_read_b64 v[198:199], %[gb] offset:896\n"
        "v_mfma_f32_16x16x32_bf16 v[108:111], v[132:135], v[152:155], v[108:111]\n"
        "ds_read_b64 v[200:201], %[ga] offset:1408\n"
        "ds_read_b64 v[202:203], %[gb] offset:1408\n"
        "v_mfma_f32_16x16x32_bf16 v[104:107], v[136:139], v[144:147], v[104:107]\n"
        "ds_read_b64 v[204:205], %[ga] offset:1920\n"
        "v_mfma_f32_16x16x32_bf16 v[108:111], v[136:139], v[148:151], v[108:111]\n"
        "ds_read_b64 v[206:207], %[gb] offset:1920\n"
        "v_mfma_f32_16x16x32_bf16 v[108:111], v[140:143], v[144:147], v[108:111]\n"
        "ds_read_b64 v[224:225], %[ga] offset:448\n"
        "ds_read_b64 v[226:227], %[gb] offset:448\n"
        "v_mfma_f32_16x16x32_bf16 v[96:99], v[160:163], v[176:179], v[96:99]\n"
        "ds_read_b128 v[240:243], %[fa] offset:4032\n"
        "v_mfma_f32_16x16x32_bf16 v[100:103], v[160:163], v[180:183], v[100:103]\n"
        "ds_read_b128 v[244:247], %[fa] offset:3520\n"
        "s_waitcnt lgkmcnt(11)\n"
        "v_mfma_f32_16x16x32_bf16 v[104:107], v[160:163], v[184:187], v[104:107]\n"
        "ds_read_b128 v[248:251], %[fa] offset:3008\n"
        "ds_read_b128 v[252:255], %[fa] offset:2496\n"
        "v_mfma_f32_16x16x32_bf16 v[108:111], v[160:163], v[188:191], v[108:111]\n"
        "ds_read_b64 v[228:229], %[ga] offset:960\n"
        "v_mfma_f32_16x16x32_bf16 v[100:103], v[164:167], v[176:179], v[100:103]\n"
        "ds_read_b64 v[230:231], %[gb] offset:960\n"
        "v_mfma_f32_16x16x32_bf16 v[104:107], v[164:167], v[180:183], v[104:107]\n"
        "ds_read_b64 v[232:233], %[ga] offset:1472\n"
        "ds_read_b64 v[234:235], %[gb] offset:1472\n"
        "v_mfma_f32_16x16x32_bf16 v[108:111], v[164:167], v[184:187], v[108:111]\n"
        "ds_read_b64 v[236:237], %[ga] offset:1984\n"
        "v_mfma_f32_16x16x32_bf16 v[104:107], v[168:171], v[176:179], v[104:107]\n"
        "ds_read_b64 v[238:239], %[gb] offset:1984\n"
        "v_mfma_f32_16x16x32_bf16 v[108:111], v[168:171], v[180:183], v[108:111]\n"
        "v_mfma_f32_16x16x32_bf16 v[108:111], v[172:175], v[176:179], v[108:111]\n"
        "v_mfma_f32_16x16x32_bf16 v[96:99], v[192:195], v[208:211], v[96:99]\n"
        "ds_read_b64 v[128:129], %[ga] offset:0\n"
        "v_mfma_f32_16x16x32_bf16 v[100:103], v[192:195], v[212:215], v[100:103]\n"
        "ds_read_b64 v[130:131], %[gb] offset:0\n"
        "v_mfma_f32_16x16x32_bf16 v[104:107], v[192:195], v[216:219], v[104:107]\n"
        "ds_read_b128 v[168:171], %[fa] offset:1536\n"
        "s_waitcnt lgkmcnt(11)\n"
        "v_mfma_f32_16x16x32_bf16 v[108:111], v[192:195], v[220:223], v[108:111]\n"
        "ds_read_b128 v[172:175], %[fa] offset:1024\n"
        "v_mfma_f32_16x16x32_bf16 v[100:103], v[196:199], v[208:211], v[100:103]\n"
        "ds_read_b64 v[132:133], %[ga] offset:512\n"
        "v_mfma_f32_16x16x32_bf16 v[104:107], v[196:199], v[212:215], v[104:107]\n"
        "ds_read_b64 v[134:135], %[gb] offset:512\n"
        "v_mfma_f32_16x16x32_bf16 v[108:111], v[196:199], v[216:219], v[108:111]\n"
        "ds_read_b128 v[164:167], %[fa] offset:2048\n"
        "v_mfma_f32_16x16x32_bf16 v[104:107], v[200:203], v[208:211], v[104:107]\n"
        "ds_read_b64 v[136:137], %[ga] offset:1024\n"
        "v_mfma_f32_16x16x32_bf16 v[108:111], v[200:203], v[212:215], v[108:111]\n"
        "ds_read_b64 v[138:139], %[gb] offset:1024\n"
        "v_mfma_f32_16x16x32_bf16 v[108:111], v[204:207], v[208:211], v[108:111]\n"
        "ds_read_b128 v[160:163], %[fa] offset:2560\n"
        "v_mfma_f32_16x16x32_bf16 v[96:99], v[224:227], v[240:243], v[96:99]\n"
        "ds_read_b64 v[140:141], %[ga] offset:1536\n"
        "v_mfma_f32_16x16x32_bf16 v[100:103], v[224:227], v[244:247], v[100:103]\n"
        "ds_read_b64 v[142:143], %[gb] offset:1536\n"
        "s_waitcnt lgkmcnt(11)\n"
        "v_mfma_f32_16x16x32_bf16 v[104:107], v[224:227], v[248:251], v[104:107]\n"
        "ds_read_b128 v[156:159], %[fa] offset:3072\n"
        "v_mfma_f32_16x16x32_bf16 v[108:111], v[224:227], v[252:255], v[108:111]\n"
        "ds_read_b64 v[144:145], %[ga] offset:2048\n"
        "v_mfma_f32_16x16x32_bf16 v[100:103], v[228:231], v[240:243], v[100:103]\n"
        "ds_read_b64 v[146:147], %[gb] offset:2048\n"
        "v_mfma_f32_16x16x32_bf16 v[104:107], v[228:231], v[244:247], v[104:107]\n"
        "ds_read_b128 v[152:155], %[fa] offset:3584\n"
        "v_mfma_f32_16x16x32_bf16 v[108:111], v[228:231], v[248:251], v[108:111]\n"
        "ds_read_b64 v[148:149], %[ga] offset:2560\n"
        "v_mfma_f32_16x16x32_bf16 v[104:107], v[232:235], v[240:243], v[104:107]\n"
        "ds_read_b64 v[150:151], %[gb] offset:2560\n"
        "v_mfma_f32_16x16x32_bf16 v[108:111], v[232:235], v[244:247], v[108:111]\n"
        "v_mfma_f32_16x16x32_bf16 v[108:111], v[236:239], v[240:243], v[108:111]\n"
        "s_waitcnt lgkmcnt(11)\n"
        "v_mfma_f32_16x16x32_bf16 v[112:115], v[128:131], v[168:171], 0\n"
        "ds_read_b64 v[192:193], %[ga] offset:64\n"
        "v_mfma_f32_16x16x32_bf16 v[116:119], v[128:131], v[172:175], 0\n"
        "ds_read_b64 v[194:195], %[gb] offset:64\n"
        "v_mfma_f32_16x16x32_bf16 v[116:119], v[132:135], v[168:171], v[116:119]\n"
        "ds_read_b128 v[232:235], %[fa] offset:1600\n"
        "v_mfma_f32_16x16x32_bf16 %[a6], v[132:135], v[172:175], 0\n"
        "ds_read_b128 v[236:239], %[fa] offset:1088\n"
        "v_mfma_f32_16x16x32_bf16 v[112:115], v[132:135], v[164:167], v[112:115]\n"
        "ds_read_b64 v[196:197], %[ga] offset:576\n"
        "s_waitcnt lgkmcnt(10)\n"
        "v_mfma_f32_16x16x32_bf16 v[116:119], v[136:139], v[164:167], v[116:119]\n"
        "ds_read_b64 v[198:199], %[gb] offset:576\n"
        "v_mfma_f32_16x16x32_bf16 %[a6], v[136:139], v[168:171], %[a6]\n"
        "ds_read_b128 v[228:231], %[fa] offset:2112\n"
        "v_cvt_pk_bf16_f32 v120, v96, v97\n"
        "v_cvt_pk_bf16_f32 v121, v98, v99\n"
        "v_mfma_f32_16x16x32_bf16 %[a7], v[136:139], v[172:175], 0\n"
        "ds_read_b64 v[200:201], %[ga] offset:1088\n"
        "ds_write_b64 %[ya], v[120:121] offset:0\n"
        "v_mfma_f32_16x16x32_bf16 v[112:115], v[136:139], v[160:163], v[112:115]\n"
        "ds_read_b64 v[202:203], %[gb] offset:1088\n"
        "v_cvt_pk_bf16_f32 v122, v100, v101\n"
        "v_cvt_pk_bf16_f32 v123, v102, v103\n"
        "v_mfma_f32_16x16x32_bf16 v[116:119], v[140:143], v[160:163], v[116:119]\n"
        "ds_read_b128 v[224:227], %[fa] offset:2624\n"
        "ds_write_b64 %[ya], v[122:123] offset:512\n"
        "v_mfma_f32_16x16x32_bf16 %[a6], v[140:143], v[164:167], %[a6]\n"
        "ds_read_b64 v[204:205], %[ga] offset:1600\n"
        "v_cvt_pk_bf16_f32 v124, v104, v105\n"
        "v_cvt_pk_bf16_f32 v125, v106, v107\n"
        "v_mfma_f32_16x16x32_bf16 %[a7], v[140:143], v[168:171], %[a7]\n"
        "ds_read_b64 v[206:207], %[gb] offset:1600\n"
        "ds_write_b64 %[ya], v[124:125] offset:1024\n"
        "v_mfma_f32_16x16x32_bf16 v[112:115], v[140:143], v[156:159], v[112:115]\n"
        "ds_read_b128 v[220:223], %[fa] offset:3136\n"
        "v_cvt_pk_bf16_f32 v126, v108, v109\n"
        "v_cvt_pk_bf16_f32 v127, v110, v111\n"
        "s_waitcnt lgkmcnt(11)\n"
        "v_mfma_f32_16x16x32_bf16 v[116:119], v[144:147], v[156:159], v[116:119]\n"
        "ds_read_b64 v[208:209], %[ga] offset:2112\n"
        "ds_write_b64 %[ya], v[126:127] offset:1536\n"
        "v_mfma_f32_16x16x32_bf16 %[a6], v[144:147], v[160:163], %[a6]\n"
        "ds_read_b64 v[210:211], %[gb] offset:2112\n"
        "v_mfma_f32_16x16x32_bf16 %[a7], v[144:147], v[164:167], %[a7]\n"
        "ds_read_b128 v[216:219], %[fa] offset:3648\n"
        "v_mfma_f32_16x16x32_bf16 v[112:115], v[144:147], v[152:155], v[112:115]\n"
        "ds_read_b64 v[212:213], %[ga] offset:2624\n"
        "v_mfma_f32_16x16x32_bf16 v[116:119], v[148:151], v[152:155], v[116:119]\n"
        "ds_read_b64 v[214:215], %[gb] offset:2624\n"
        "v_mfma_f32_16x16x32_bf16 %[a6], v[148:151], v[156:159], %[a6]\n"
        "v_mfma_f32_16x16x32_bf16 %[a7], v[148:151], v[160:163], %[a7]\n"
        "s_waitcnt lgkmcnt(4)\n"
        "s_barrier\n"
        "s_waitcnt lgkmcnt(8)\n"
        "s_mov_b32 %[cnt], 3\n"
        "2:\n"
        "v_mfma_f32_16x16x32_bf16 v[112:115], v[192:195], v[232:235], v[112:115]\n"
        "ds_read_b64 v[128:129], %[ga] offset:128\n"
        "v_mfma_f32_16x16x32_bf16 v[116:119], v[192:195], v[236:239], v[116:119]\n"
        "ds_read_b64 v[130:131], %[gb] offset:128\n"
        "v_mfma_f32_16x16x32_bf16 v[116:119], v[196:199], v[232:235], v[116:119]\n"
        "ds_read_b128 v[168:171], %[fa] offset:1664\n"
        "v_mfma_f32_16x16x32_bf16 %[a6], v[196:199], v[236:239], %[a6]\n"
        "ds_read_b128 v[172:175], %[fa] offset:1152\n"
        "v_mfma_f32_16x16x32_bf16 v[112:115], v[196:199], v[228:231], v[112:115]\n"
        "ds_read_b64 v[132:133], %[ga] offset:640\n"
        "v_mfma_f32_16x16x32_bf16 v[116:119], v[200:203], v[228:231], v[116:119]\n"
        "ds_read_b64 v[134:135], %[gb] offset:640\n"
        "v_mfma_f32_16x16x32_bf16 %[a6], v[200:203], v[232:235], %[a6]\n"
        "ds_read_b128 v[164:167], %[fa] offset:2176\n"
        "v_mfma_f32_16x16x32_bf16 %[a7], v[200:203], v[236:239], %[a7]\n"
        "ds_read_b64 v[136:137], %[ga] offset:1152\n"
        "v_mfma_f32_16x16x32_bf16 v[112:115], v[200:203], v[224:227], v[112:115]\n"
        "ds_read_b64 v[138:139], %[gb] offset:1152\n"
        "v_mfma_f32_16x16x32_bf16 v[116:119], v[204:207], v[224:227], v[116:119]\n"
        "ds_read_b128 v[160:163], %[fa] offset:2688\n"
        "v_mfma_f32_16x16x32_bf16 %[a6], v[204:207], v[228:231], %[a6]\n"
        "ds_read_b64 v[140:141], %[ga] offset:1664\n"
        "v_mfma_f32_16x16x32_bf16 %[a7], v[204:207], v[232:235], %[a7]\n"
        "ds_read_b64 v[142:143], %[gb] offset:1664\n"
        "s_waitcnt lgkmcnt(11)\n"
        "v_mfma_f32_16x16x32_bf16 v[112:115], v[204:207], v[220:223], v[112:115]\n"
        "ds_read_b128 v[156:159], %[fa] offset:3200\n"
        "v_mfma_f32_16x16x32_bf16 v[116:119], v[208:211], v[220:223], v[116:119]\n"
        "ds_read_b64 v[144:145], %[ga] offset:2176\n"
        "v_mfma_f32_16x16x32_bf16 %[a6], v[208:211], v[224:227], %[a6]\n"
        "ds_read_b64 v[146:147], %[gb] offset:2176\n"
        "v_mfma_f32_16x16x32_bf16 %[a7], v[208:211], v[228:231], %[a7]\n"
        "ds_read_b128 v[152:155], %[fa] offset:3712\n"
        "v_mfma_f32_16x16x32_bf16 v[112:115], v[208:211], v[216:219], v[112:115]\n"
        "ds_read_b64 v[148:149], %[ga] offset:2688\n"
        "v_mfma_f32_16x16x32_bf16 v[116:119], v[212:215], v[216:219], v[116:119]\n"
        "ds_read_b64 v[150:151], %[gb] offset:2688\n"
        "v_mfma_f32_16x16x32_bf16 %[a6], v[212:215], v[220:223], %[a6]\n"
        "v_mfma_f32_16x16x32_bf16 %[a7], v[212:215], v[224:227], %[a7]\n"
        "s_waitcnt lgkmcnt(11)\n"
        "v_mfma_f32_16x16x32_bf16 v[112:115], v[128:131], v[168:171], v[112:115]\n"
        "ds_read_b64 v[192:193], %[ga] offset:192\n"
        "v_mfma_f32_16x16x32_bf16 v[116:119], v[128:131], v[172:175], v[116:119]\n"
        "ds_read_b64 v[194:195], %[gb] offset:192\n"
        "v_mfma_f32_16x16x32_bf16 v[116:119], v[132:135], v[168:171], v[116:119]\n"
        "ds_read_b128 v[232:235], %[fa] offset:1728\n"
        "v_mfma_f32_16x16x32_bf16 %[a6], v[132:135], v[172:175], %[a6]\n"
        "ds_read_b128 v[236:239], %[fa] offset:1216\n"
        "v_mfma_f32_16x16x32_bf16 v[112:115], v[132:135], v[164:167], v[112:115]\n"
        "ds_read_b64 v[196:197], %[ga] offset:704\n"
        "s_waitcnt lgkmcnt(10)\n"
        "v_mfma_f32_16x16x32_bf16 v[116:119], v[136:139], v[164:167], v[116:119]\n"
        "ds_read_b64 v[198:199], %[gb] offset:704\n"
        "v_mfma_f32_16x16x32_bf16 %[a6], v[136:139], v[168:171], %[a6]\n"
        "ds_read_b128 v[228:231], %[fa] offset:2240\n"
        "v_mfma_f32_16x16x32_bf16 %[a7], v[136:139], v[172:175], %[a7]\n"
        "ds_read_b64 v[200:201], %[ga] offset:1216\n"
        "v_mfma_f32_16x16x32_bf16 v[112:115], v[136:139], v[160:163], v[112:115]\n"
        "ds_read_b64 v[202:203], %[gb] offset:1216\n"
        "v_mfma_f32_16x16x32_bf16 v[116:119], v[140:143], v[160:163], v[116:119]\n"
        "ds_read_b128 v[224:227], %[fa] offset:2752\n"
        "v_mfma_f32_16x16x32_bf16 %[a6], v[140:143], v[164:167], %[a6]\n"
        "ds_read_b64 v[204:205], %[ga] offset:1728\n"
        "v_mfma_f32_16x16x32_bf16 %[a7], v[140:143], v[168:171], %[a7]\n"
        "ds_read_b64 v[206:207], %[gb] offset:1728\n"
        "v_mfma_f32_16x16x32_bf16 v[112:115], v[140:143], v[156:159], v[112:115]\n"
        "ds_read_b128 v[220:223], %[fa] offset:3264\n"
        "s_waitcnt lgkmcnt(11)\n"
        "v_mfma_f32_16x16x32_bf16 v[116:119], v[144:147], v[156:159], v[116:119]\n"
        "ds_read_b64 v[208:209], %[ga] offset:2240\n"
        "v_mfma_f32_16x16x32_bf16 %[a6], v[144:147], v[160:163], %[a6]\n"
        "ds_read_b64 v[210:211], %[gb] offset:2240\n"
        "v_mfma_f32_16x16x32_bf16 %[a7], v[144:147], v[164:167], %[a7]\n"
        "ds_read_b128 v[216:219], %[fa] offset:3776\n"
        "v_mfma_f32_16x16x32_bf16 v[112:115], v[144:147], v[152:155], v[112:115]\n"
        "ds_read_b64 v[212:213], %[ga] offset:2752\n"
        "v_mfma_f32_16x16x32_bf16 v[116:119], v[148:151], v[152:155], v[116:119]\n"
        "ds_read_b64 v[214:215], %[gb] offset:2752\n"
        "v_mfma_f32_16x16x32_bf16 %[a6], v[148:151], v[156:159], %[a6]\n"
        "v_mfma_f32_16x16x32_bf16 %[a7], v[148:151], v[160:163], %[a7]\n"
        "v_add_u32 %[ga], 128, %[ga]\n"
        "v_add_u32 %[gb], 128, %[gb]\n"
        "v_add_u32 %[fa], 128, %[fa]\n"
        "s_waitcnt lgkmcnt(8)\n"
        "s_sub_u32 %[cnt], %[cnt], 1\n"
        "s_cmp_lg_u32 %[cnt], 0\n"
        "s_cbranch_scc1 2b\n"
        "v_subrev_u32 %[ga], 384, %[ga]\n"
        "v_subrev_u32 %[gb], 384, %[gb]\n"
        "v_subrev_u32 %[fa], 384, %[fa]\n"
        "v_mfma_f32_16x16x32_bf16 v[112:115], v[192:195], v[232:235], v[112:115]\n"
        "ds_read_b64 v[128:129], %[ga] offset:0\n"
        "ds_read_b64 v[130:131], %[gb] offset:0\n"
        "v_mfma_f32_16x16x32_bf16 v[116:119], v[192:195], v[236:239], v[116:119]\n"
        "ds_read_b128 v[152:155], %[fa] offset:512\n"
        "v_mfma_f32_16x16x32_bf16 v[116:119], v[196:199], v[232:235], v[116:119]\n"
        "ds_read_b128 v[156:159], %[fa] offset:0\n"
        "v_mfma_f32_16x16x32_bf16 %[a6], v[196:199], v[236:239], %[a6]\n"
        "ds_read_b64 v[132:133], %[ga] offset:512\n"
        "ds_read_b64 v[134:135], %[gb] offset:512\n"
        "v_mfma_f32_16x16x32_bf16 v[112:115], v[196:199], v[228:231], v[112:115]\n"
        "ds_read_b64 v[136:137], %[ga] offset:3072\n"
        "v_mfma_f32_16x16x32_bf16 v[116:119], v[200:203], v[228:231], v[116:119]\n"
        "ds_read_b64 v[138:139], %[gb] offset:3072\n"
        "v_mfma_f32_16x16x32_bf16 %[a6], v[200:203], v[232:235], %[a6]\n"
        "ds_read_b128 v[144:147], %[fa] offset:3584\n"
        "ds_read_b128 v[148:151], %[fa] offset:3072\n"
        "v_mfma_f32_16x16x32_bf16 %[a7], v[200:203], v[236:239], %[a7]\n"
        "ds_read_b64 v[140:141], %[ga] offset:3584\n"
        "v_mfma_f32_16x16x32_bf16 v[112:115], v[200:203], v[224:227], v[112:115]\n"
        "ds_read_b64 v[142:143], %[gb] offset:3584\n"
        "s_waitcnt lgkmcnt(11)\n"
        "v_mfma_f32_16x16x32_bf16 v[116:119], v[204:207], v[224:227], v[116:119]\n"
        "ds_read_b64 v[160:161], %[ga] offset:64\n"
        "ds_read_b64 v[162:163], %[gb] offset:64\n"
        "v_mfma_f32_16x16x32_bf16 %[a6], v[204:207], v[228:231], %[a6]\n"
        "ds_read_b128 v[184:187], %[fa] offset:576\n"
        "v_mfma_f32_16x16x32_bf16 %[a7], v[204:207], v[232:235], %[a7]\n"
        "ds_read_b128 v[188:191], %[fa] offset:64\n"
        "v_mfma_f32_16x16x32_bf16 v[112:115], v[204:207], v[220:223], v[112:115]\n"
        "ds_read_b64 v[164:165], %[ga] offset:576\n"
        "ds_read_b64 v[166:167], %[gb] offset:576\n"
        "v_mfma_f32_16x16x32_bf16 v[116:119], v[208:211], v[220:223], v[116:119]\n"
        "ds_read_b64 v[168:169], %[ga] offset:3136\n"
        "v_mfma_f32_16x16x32_bf16 %[a6], v[208:211], v[224:227], %[a6]\n"
        "ds_read_b64 v[170:171], %[gb] offset:3136\n"
        "v_mfma_f32_16x16x32_bf16 %[a7], v[208:211], v[228:231], %[a7]\n"
        "ds_read_b128 v[176:179], %[fa] offset:3648\n"
        "ds_read_b128 v[180:183], %[fa] offset:3136\n"
        "v_mfma_f32_16x16x32_bf16 v[112:115], v[208:211], v[216:219], v[112:115]\n"
        "ds_read_b64 v[172:173], %[ga] offset:3648\n"
        "v_mfma_f32_16x16x32_bf16 v[116:119], v[212:215], v[216:219], v[116:119]\n"
        "ds_read_b64 v[174:175], %[gb] offset:3648\n"
        "v_mfma_f32_16x16x32_bf16 %[a6], v[212:215], v[220:223], %[a6]\n"
        "v_mfma_f32_16x16x32_bf16 %[a7], v[212:215], v[224:227], %[a7]\n"
        "s_waitcnt lgkmcnt(11)\n"
        "v_mfma_f32_16x16x32_bf16 %[a6], v[128:131], v[152:155], %[a6]\n"
        "ds_read_b64 v[192:193], %[ga] offset:128\n"
        "ds_read_b64 v[194:195], %[gb] offset:128\n"
        "ds_read_b128 v[216:219], %[fa] offset:640\n"
        "v_mfma_f32_16x16x32_bf16 %[a7], v[128:131], v[156:159], %[a7]\n"
        "ds_read_b128 v[220:223], %[fa] offset:128\n"
        "ds_read_b64 v[196:197], %[ga] offset:640\n"
        "v_mfma_f32_16x16x32_bf16 %[a7], v[132:135], v[152:155], %[a7]\n"
        "ds_read_b64 v[198:199], %[gb] offset:640\n"
        "ds_read_b64 v[200:201], %[ga] offset:3200\n"
        "ds_read_b64 v[202:203], %[gb] offset:3200\n"
        "v_mfma_f32_16x16x32_bf16 %[a6], v[136:139], v[144:147], %[a6]\n"
        "ds_read_b128 v[208:211], %[fa] offset:3712\n"
        "ds_read_b128 v[212:215], %[fa] offset:3200\n"
        "v_mfma_f32_16x16x32_bf16 %[a7], v[136:139], v[148:151], %[a7]\n"
        "ds_read_b64 v[204:205], %[ga] offset:3712\n"
        "ds_read_b64 v[206:207], %[gb] offset:3712\n"
        "v_mfma_f32_16x16x32_bf16 %[a7], v[140:143], v[144:147], %[a7]\n"
        "ds_read_b64 v[224:225], %[ga] offset:192\n"
        "ds_read_b64 v[226:227], %[gb] offset:192\n"
        "ds_read_b128 v[248:251], %[fa] offset:704\n"
        "s_waitcnt lgkmcnt(11)\n"
        "v_mfma_f32_16x16x32_bf16 %[a6], v[160:163], v[184:187], %[a6]\n"
        "ds_read_b128 v[252:255], %[fa] offset:192\n"
        "ds_read_b64 v[228:229], %[ga] offset:704\n"
        "v_cvt_pk_bf16_f32 v120, v112, v113\n"
        "v_cvt_pk_bf16_f32 v121, v114, v115\n"
        "v_mfma_f32_16x16x32_bf16 %[a7], v[160:163], v[188:191], %[a7]\n"
        "ds_read_b64 v[230:231], %[gb] offset:704\n"
        "ds_read_b64 v[232:233], %[ga] offset:3264\n"
        "ds_read_b64 v[234:235], %[gb] offset:3264\n"
        "ds_write_b64 %[ya], v[120:121] offset:2048\n"
        "v_mfma_f32_16x16x32_bf16 %[a7], v[164:167], v[184:187], %[a7]\n"
        "ds_read_b128 v[240:243], %[fa] offset:3776\n"
        "ds_read_b128 v[244:247], %[fa] offset:3264\n"
        "v_cvt_pk_bf16_f32 v122, v116, v117\n"
        "v_cvt_pk_bf16_f32 v123, v118, v119\n"
        "v_mfma_f32_16x16x32_bf16 %[a6], v[168:171], v[176:179], %[a6]\n"
        "ds_read_b64 v[236:237], %[ga] offset:3776\n"
        "ds_read_b64 v[238:239], %[gb] offset:3776\n"
        "ds_write_b64 %[ya], v[122:123] offset:2560\n"
        "v_mfma_f32_16x16x32_bf16 %[a7], v[168:171], v[180:183], %[a7]\n"
        "v_mfma_f32_16x16x32_bf16 %[a7], v[172:175], v[176:179], %[a7]\n"
        "s_waitcnt lgkmcnt(0)\n"
        "s_barrier\n"
        "v_mfma_f32_16x16x32_bf16 %[a6], v[192:195], v[216:219], %[a6]\n"
        "ds_read_b64 v[128:129], %[ga] offset:256\n"
        "ds_read_b64 v[130:131], %[gb] offset:256\n"
        "ds_read_b128 v[152:155], %[fa] offset:768\n"
        "v_mfma_f32_16x16x32_bf16 %[a7], v[192:195], v[220:223], %[a7]\n"
        "ds_read_b128 v[156:159], %[fa] offset:256\n"
        "ds_read_b64 v[132:133], %[ga] offset:768\n"
        "v_mfma_f32_16x16x32_bf16 %[a7], v[196:199], v[216:219], %[a7]\n"
        "ds_read_b64 v[134:135], %[gb] offset:768\n"
        "ds_read_b64 v[136:137], %[ga] offset:3328\n"
        "ds_read_b64 v[138:139], %[gb] offset:3328\n"
        "v_mfma_f32_16x16x32_bf16 %[a6], v[200:203], v[208:211], %[a6]\n"
        "ds_read_b128 v[144:147], %[fa] offset:3840\n"
        "ds_read_b128 v[148:151], %[fa] offset:3328\n"
        "v_mfma_f32_16x16x32_bf16 %[a7], v[200:203], v[212:215], %[a7]\n"
        "ds_read_b64 v[140:141], %[ga] offset:3840\n"
        "ds_read_b64 v[142:143], %[gb] offset:3840\n"
        "v_mfma_f32_16x16x32_bf16 %[a7], v[204:207], v[208:211], %[a7]\n"
        "ds_read_b64 v[160:161], %[ga] offset:320\n"
        "ds_read_b64 v[162:163], %[gb] offset:320\n"
        "ds_read_b128 v[184:187], %[fa] offset:832\n"
        "v_mfma_f32_16x16x32_bf16 %[a6], v[224:227], v[248:251], %[a6]\n"
        "ds_read_b128 v[188:191], %[fa] offset:320\n"
        "ds_read_b64 v[164:165], %[ga] offset:832\n"
        "v_mfma_f32_16x16x32_bf16 %[a7], v[224:227], v[252:255], %[a7]\n"
        "ds_read_b64 v[166:167], %[gb] offset:832\n"
        "ds_read_b64 v[168:169], %[ga] offset:3392\n"
        "ds_read_b64 v[170:171], %[gb] offset:3392\n"
        "v_mfma_f32_16x16x32_bf16 %[a7], v[228:231], v[248:251], %[a7]\n"
        "ds_read_b128 v[176:179], %[fa] offset:3904\n"
        "ds_read_b128 v[180:183], %[fa] offset:3392\n"
        "v_mfma_f32_16x16x32_bf16 %[a6], v[232:235], v[240:243], %[a6]\n"
        "ds_read_b64 v[172:173], %[ga] offset:3904\n"
        "ds_read_b64 v[174:175], %[gb] offset:3904\n"
        "v_mfma_f32_16x16x32_bf16 %[a7], v[232:235], v[244:247], %[a7]\n"
        "v_mfma_f32_16x16x32_bf16 %[a7], v[236:239], v[240:243], %[a7]\n"
        "s_waitcnt lgkmcnt(11)\n"
        "v_mfma_f32_16x16x32_bf16 %[a6], v[128:131], v[152:155], %[a6]\n"
        "ds_read_b64 v[192:193], %[ga] offset:384\n"
        "ds_read_b64 v[194:195], %[gb] offset:384\n"
        "ds_read_b128 v[216:219], %[fa] offset:896\n"
        "v_mfma_f32_16x16x32_bf16 %[a7], v[128:131], v[156:159], %[a7]\n"
        "ds_read_b128 v[220:223], %[fa] offset:384\n"
        "ds_read_b64 v[196:197], %[ga] offset:896\n"
        "v_mfma_f32_16x16x32_bf16 %[a7], v[132:135], v[152:155], %[a7]\n"
        "ds_read_b64 v[198:199], %[gb] offset:896\n"
        "ds_read_b64 v[200:201], %[ga] offset:3456\n"
        "ds_read_b64 v[202:203], %[gb] offset:3456\n"
        "v_mfma_f32_16x16x32_bf16 %[a6], v[136:139], v[144:147], %[a6]\n"
        "ds_read_b128 v[208:211], %[fa] offset:3968\n"
        "ds_read_b128 v[212:215], %[fa] offset:3456\n"
        "v_mfma_f32_16x16x32_bf16 %[a7], v[136:139], v[148:151], %[a7]\n"
        "ds_read_b64 v[204:205], %[ga] offset:3968\n"
        "ds_read_b64 v[206:207], %[gb] offset:3968\n"
        "v_mfma_f32_16x16x32_bf16 %[a7], v[140:143], v[144:147], %[a7]\n"
        "ds_read_b64 v[224:225], %[ga] offset:448\n"
        "ds_read_b64 v[226:227], %[gb] offset:448\n"
        "ds_read_b128 v[248:251], %[fa] offset:960\n"
        "s_waitcnt lgkmcnt(11)\n"
        "v_mfma_f32_16x16x32_bf16 %[a6], v[160:163], v[184:187], %[a6]\n"
        "ds_read_b128 v[252:255], %[fa] offset:448\n"
        "ds_read_b64 v[228:229], %[ga] offset:960\n"
        "v_mfma_f32_16x16x32_bf16 %[a7], v[160:163], v[188:191], %[a7]\n"
        "ds_read_b64 v[230:231], %[gb] offset:960\n"
        "ds_read_b64 v[232:233], %[ga] offset:3520\n"
        "ds_read_b64 v[234:235], %[gb] offset:3520\n"
        "v_mfma_f32_16x16x32_bf16 %[a7], v[164:167], v[184:187], %[a7]\n"
        "ds_read_b128 v[240:243], %[fa] offset:4032\n"
        "ds_read_b128 v[244:247], %[fa] offset:3520\n"
        "v_mfma_f32_16x16x32_bf16 %[a6], v[168:171], v[176:179], %[a6]\n"
        "ds_read_b64 v[236:237], %[ga] offset:4032\n"
        "ds_read_b64 v[238:239], %[gb] offset:4032\n"
        "v_mfma_f32_16x16x32_bf16 %[a7], v[168:171], v[180:183], %[a7]\n"
        "v_mfma_f32_16x16x32_bf16 %[a7], v[172:175], v[176:179], %[a7]\n"
        "v_mfma_f32_16x16x32_bf16 %[a6], v[192:195], v[216:219], %[a6]\n"
        "v_mfma_f32_16x16x32_bf16 %[a7], v[192:195], v[220:223], %[a7]\n"
        "s_waitcnt lgkmcnt(11)\n"
        "v_mfma_f32_16x16x32_bf16 %[a7], v[196:199], v[216:219], %[a7]\n"
        "v_mfma_f32_16x16x32_bf16 %[a6], v[200:203], v[208:211], %[a6]\n"
        "v_mfma_f32_16x16x32_bf16 %[a7], v[200:203], v[212:215], %[a7]\n"
        "v_mfma_f32_16x16x32_bf16 %[a7], v[204:207], v[208:211], %[a7]\n"
        "s_waitcnt lgkmcnt(8)\n"
        "v_mfma_f32_16x16x32_bf16 %[a6], v[224:227], v[248:251], %[a6]\n"
        "v_mfma_f32_16x16x32_bf16 %[a7], v[224:227], v[252:255], %[a7]\n"
        "s_waitcnt lgkmcnt(6)\n"
        "v_mfma_f32_16x16x32_bf16 %[a7], v[228:231], v[248:251], %[a7]\n"
        "s_waitcnt lgkmcnt(3)\n"
        "v_mfma_f32_16x16x32_bf16 %[a6], v[232:235], v[240:243], %[a6]\n"
        "s_waitcnt lgkmcnt(2)\n"
        "v_mfma_f32_16x16x32_bf16 %[a7], v[232:235], v[244:247], %[a7]\n"
        "s_waitcnt lgkmcnt(0)\n"
        "v_mfma_f32_16x16x32_bf16 %[a7], v[236:239], v[240:243], %[a7]\n"
        "s_setprio 0\n"
        "s_nop 7\n"
        "s_nop 1\n"
        : [ga] "+v"(ga), [gb] "+v"(gb), [fa] "+v"(fa), [a6] "=&v"(acc[0]), [a7] "=&v"(acc[1]), [cnt] "=&s"(cnt)
        : [ya] "v"(ya)
        : "memory", "scc", "v96", "v97", "v98", "v99", "v100", "v101", "v102", "v103", "v104", "v105", "v106", "v107", "v108", "v109", "v110", "v111", "v112", "v113", "v114", "v115", "v116", "v117", "v118", "v119", "v120", "v121", "v122", "v123", "v124", "v125", "v126", "v127", "v128", "v129", "v130", "v131", "v132", "v133", "v134", "v135", "v136", "v137", "v138", "v139", "v140", "v141", "v142", "v143", "v144", "v145", "v146", "v147", "v148", "v149", "v150", "v151", "v152", "v153", "v154", "v155", "v156", "v157", "v158", "v159", "v160", "v161", "v162", "v163", "v164", "v165", "v166", "v167", "v168", "v169", "v170", "v171", "v172", "v173", "v174", "v175", "v176", "v177", "v178", "v179", "v180", "v181", "v182", "v183", "v184", "v185", "v186", "v187", "v188", "v189", "v190", "v191", "v192", "v193", "v194", "v195", "v196", "v197", "v198", "v199", "v200", "v201", "v202", "v203", "v204", "v205", "v206", "v207", "v208", "v209", "v210", "v211", "v212", "v213", "v214", "v215", "v216", "v217", "v218", "v219", "v220", "v221", "v222", "v223", "v224", "v225", "v226", "v227", "v228", "v229", "v230", "v231", "v232", "v233", "v234", "v235", "v236", "v237", "v238", "v239", "v240", "v241", "v242", "v243", "v244", "v245", "v246", "v247", "v248", "v249", "v250", "v251", "v252", "v253", "v254", "v255");
}

__device__ __forceinline__ float dot2(u32 a, u32 b, float c) {
    return __builtin_amdgcn_fdot2_f32_bf16(__builtin_bit_cast(bf16x2, a), __builtin_bit_cast(bf16x2, b), c, false);
}
__device__ __forceinline__ void ktilde_job(int kc, int rt, int lane, u32x2 xt, float tap0) {
    const int r = lane & 15, q = lane >> 4;
    const int ab = L_X + kc * TB_CH + (16 * rt + r) * TB_ROW + 16 * q, bb = L_X + kc * TB_CH + (48 + r) * TB_ROW + 16 * q;
    u32x4 A[8], B[8];
#pragma unroll
    for (int t = 0; t < 8; ++t) { A[t] = lds_ld<u32x4>(ab + 64 * t); B[t] = lds_ld<u32x4>(bb + 64 * t); }
    asm volatile("" : "+v"(A[0]), "+v"(A[1]), "+v"(A[2]), "+v"(A[3]), "+v"(A[4]), "+v"(A[5]), "+v"(A[6]), "+v"(A[7]),
                      "+v"(B[0]), "+v"(B[1]), "+v"(B[2]), "+v"(B[3]), "+v"(B[4]), "+v"(B[5]), "+v"(B[6]), "+v"(B[7]));
    f32x4 acc0 = {0, 0, 0, 0}, acc1 = {0, 0, 0, 0};
#pragma unroll
    for (int t = 0; t < 8; t += 2) {
        acc0 = __builtin_amdgcn_mfma_f32_16x16x32_bf16(__builtin_bit_cast(bf16x8, A[t]), __builtin_bit_cast(bf16x8, B[t]), acc0, 0, 0, 0);
        acc1 = __builtin_amdgcn_mfma_f32_16x16x32_bf16(__builtin_bit_cast(bf16x8, A[t + 1]), __builtin_bit_cast(bf16x8, B[t + 1]), acc1, 0, 0, 0);
    }
    const int kb = L_KB + kc * 1312;
    lds_st<u16>(kb + (xt[0] & 0xffff), (u16)pk(acc0[0] + acc1[0] + tap0, 0.f));
    lds_st<u16>(kb + (xt[0] >> 16), (u16)pk(acc0[1] + acc1[1], 0.f));
    lds_st<u16>(kb + (xt[1] & 0xffff), (u16)pk(acc0[2] + acc1[2], 0.f));
    lds_st<u16>(kb + (xt[1] >> 16), (u16)pk(acc0[3] + acc1[3], 0.f));
}
__device__ __forceinline__ void expand_chunk(int ch, int j, u32x4 gt0, u32x2 gt1) {
    const int kb = L_KB + ch * 1312;
    u32 e[11];
#pragma unroll
    for (int m = 0; m < 11; ++m) {
        const u32 t = m < 8 ? gt0[m >> 1] : gt1[(m - 8) >> 1];
        e[m] = lds_ld<u16>(kb + ((m & 1) ? (t >> 16) : (t & 0xffff)));
    }
    write_copies(L_X + ch * KT_CC, j, e[0] | (e[1] << 16), e[2] | (e[3] << 16), e[4] | (e[5] << 16), e[6] | (e[7] << 16), e[8] | (e[9] << 16), e[10]);
}

constexpr int C_PUB = L_MISC, C_TB = L_MISC + 4, C_KB = L_MISC + 8, C_KT = L_MISC + 12, C_GO = L_MISC + 16, C_GO2 = L_MISC + 20;

__global__ void __launch_bounds__(NT, 2) fused_kernel(Params P) {
    extern __shared__ __attribute__((aligned(16))) char smem_[];
    const int tid = threadIdx.x, lane = tid & 63;
    const int wv = __builtin_amdgcn_readfirstlane(tid >> 6);
    const int w = blockIdx.x, grp = w & 7, ti = w >> 3;
    const int d0 = 64 * grp + 2 * ti;
    char* ws = P.ws;
    asm volatile("" :: "s"(P.q), "s"(P.k), "s"(P.v), "s"(P.sk), "s"(P.ssm), "s"(P.sD), "s"(P.are), "s"(P.aim), "s"(P.bre), "s"(P.bim), "s"(P.cre), "s"(P.cim),
                       "s"(P.ldt), "s"(P.dsk), "s"(P.gw), "s"(P.out), "s"(P.ws));
    const __amdgpu_buffer_rsrc_t sig_rsrc = __builtin_amdgcn_make_buffer_rsrc(ws + WS_SIG, 0, 2 * D * S * 2, 0x00020000);
    const __amdgpu_buffer_rsrc_t g_rsrc = __builtin_amdgcn_make_buffer_rsrc(ws + WS_G, 0, S * 4, 0x00020000);

    u32x4 t3[3], gq[2], lh, gwv, sm[2], gt0;
    u32x2 skw, sm2[2], sme, gt1, xt0;
    u32 pl, pa, pb, pc, pd, pe, pf, sd0, dke;
    u64 tag;
    const bool has_halo = (wv == 0) && (ti > 0);
    const int kw = wv - 4, kt = tid - 256, kch = kw >> 1, kj = tid & 255;
#define GLD(T, base, off) (*(const GAS T*)((const GAS char*)(base) + (u32)(off)))
    {
        const u32 toff = (u32)((64 * ti + 8 * wv) * D + 64 * grp) * 2u + (u32)(lane >> 3) * (D * 2) + (u32)(lane & 7) * 16u;
        const u32 soff = (u32)(64 * grp + 8 * wv) * 8u + (u32)(lane >> 3) * 8u;
        if (has_halo) asm volatile("global_load_dwordx4 %0, %1, %2" : "=&v"(lh) : "v"(toff - 8 * D * 2), "s"(P.k) : "memory");
        else asm volatile("" : "=v"(lh));
        asm volatile("global_load_dwordx2 %3, %5, %9\n\t"
                     "global_load_dwordx4 %0, %4, %6\n\tglobal_load_dwordx4 %1, %4, %7\n\tglobal_load_dwordx4 %2, %4, %8"
                     : "=&v"(t3[0]), "=&v"(t3[1]), "=&v"(t3[2]), "=&v"(skw)
                     : "v"(toff), "v"(soff), "s"(P.k), "s"(P.v), "s"(P.q), "s"(P.sk) : "memory");
        __builtin_amdgcn_sched_barrier(0);
    }
    asm volatile("" : "=v"(gq[0]), "=v"(gq[1]), "=v"(gwv), "=v"(sm[0]), "=v"(sm[1]), "=v"(sm2[0]), "=v"(sm2[1]), "=v"(sme), "=v"(sd0));
    asm volatile("" : "=v"(pl), "=v"(pa), "=v"(pb), "=v"(pc), "=v"(pd), "=v"(pe), "=v"(pf), "=v"(dke));
    if (wv >= 4) {
        const u32 poff = (u32)((d0 + kch) * NS + 64 * (kw & 1)) * 2u + (u32)lane * 2u;
        pa = GLD(u16, P.are, poff); pb = GLD(u16, P.aim, poff); pc = GLD(u16, P.bre, poff); pd = GLD(u16, P.bim, poff); pe = GLD(u16, P.cre, poff); pf = GLD(u16, P.cim, poff);
        pl = P.ldt[d0 + kch];
        dke = P.dsk[d0 + (kw == 3 ? 1 : 0)];
        xt0 = GLD(u32x2, &idx_tabs.x[kw % 3][0][0], lane * 8);
    } else {
        gq[0] = GLD(u32x4, P.q + (size_t)(8 * w + 2 * wv) * D, lane * 16); gq[1] = GLD(u32x4, P.q + (size_t)(8 * w + 2 * wv + 1) * D, lane * 16);
        gwv = GLD(u32x4, P.gw, lane * 16);
        const u32 koff = (u32)kj * 16u, koff2 = koff + (kj < 255 ? 16u : 0u);
        sm[0] = GLD(u32x4, P.ssm + (size_t)d0 * S, koff); sm2[0] = GLD(u32x2, P.ssm + (size_t)d0 * S, koff2);
        sm[1] = GLD(u32x4, P.ssm + (size_t)(d0 + 1) * S, koff); sm2[1] = GLD(u32x2, P.ssm + (size_t)(d0 + 1) * S, koff2);
        sme = GLD(u32x2, P.ssm + (size_t)d0 * S, ((lane >> 2) & 1) * (S * 2));
        sd0 = GLD(u32, P.sD + d0, 0);
        xt0 = GLD(u32x2, &idx_tabs.x[wv == 3 ? 2 : 1][0][0], lane * 8);
    }
    tag = __hip_atomic_load((const gu64*)(ws + WS_EPOCH), __ATOMIC_RELAXED, __HIP_MEMORY_SCOPE_AGENT);
    gt0 = GLD(u32x4, &idx_tabs.g[0][0], kj * 24);
    gt1 = GLD(u32x2, &idx_tabs.g[0][8], kj * 24);
    u32 pfv;
    {
        u64 pc; u32 len;
        asm volatile("s_getpc_b64 %0\n.Lfk_pf_start:\n\ts_mov_b32 %1, .Lfk_pf_end-.Lfk_pf_start" : "=s"(pc), "=s"(len));
        u32 off = (u32)kj * 128u;
        off = (wv >= 4 && off < len - 4u) ? off : 0u;
        pfv = *(const GAS u32*)(pc + (off & ~3u));
    }
    if (tid >= 448 && tid < 450) lds_st<u32x4>(L_MISC + 16 * (tid - 448), (u32x4){0, 0, 0, 0});
    if (tid >= 64 && tid < 64 + 4 * 30) lds_st<u32x4>(L_SIG + ((tid - 64) / 30) * SIG_ST + 4096 + ((tid - 64) % 30) * 16, (u32x4){0, 0, 0, 0});
    __builtin_amdgcn_sched_barrier(0);

    asm volatile("s_waitcnt vmcnt(11)" : "+v"(t3[0]), "+v"(t3[1]), "+v"(t3[2]), "+v"(lh), "+v"(skw));
    __builtin_amdgcn_sched_barrier(0);
    {
        const int r = lane >> 3, o = lane & 7;
        const u32 sel = (r & 1) ? 0x07060302u : 0x01000504u;
        const int c = 8 * o + r;
        const int a = L_T + c * 128 + (((7 - wv) ^ o) << 4);
        tr8n<3>(t3, sel);
        lds_st<u32x4>(a + 8192, t3[0]);
        lds_st<u32x4>(a + 16384, t3[1]);
        lds_st<u32x4>(a, t3[2]);
        if (wv == 0) { u32x4 h1[1] = {lh}; tr8n<1>(h1, sel); if (!has_halo) h1[0] = (u32x4){0, 0, 0, 0}; lds_st<u32x4>(L_H + c * 16, h1[0]); }
    }
    __syncthreads();
    {
        const int oct = lane & 7, c = 8 * wv + (lane >> 3);
        const int a = L_T + c * 128 + ((oct ^ wv) << 4);
        const u32x4 tk = lds_ld<u32x4>(a + 8192);
        const int an = oct < 7 ? (L_T + 8192 + c * 128 + (((oct + 1) ^ wv) << 4)) : (L_H + c * 16);
        const u32x2 tn = lds_ld<u32x2>(an);
        const u32x4 tv = lds_ld<u32x4>(a + 16384), tq = lds_ld<u32x4>(a);
        const u32 go = ((64 * grp + c) * S + 1984 - 64 * ti + 8 * oct) * 2;
        __builtin_amdgcn_raw_buffer_store_b128(tq, sig_rsrc, go + D * S * 2, 0, 16);
        const u32 dd[6] = {tk[0], tk[1], tk[2], tk[3], tn[0], tn[1]};
        float kv[8];
#pragma unroll
        for (int m = 0; m < 4; ++m) {
            const u32 o0 = __builtin_amdgcn_alignbit(dd[m + 1], dd[m], 16), o1 = __builtin_amdgcn_alignbit(dd[m + 2], dd[m + 1], 16);
            kv[2 * m] = dot2(dd[m], skw[0], dot2(dd[m + 1], skw[1], 0.f)) * lo2f(tv[m]);
            kv[2 * m + 1] = dot2(o0, skw[0], dot2(o1, skw[1], 0.f)) * hi2f(tv[m]);
        }
        const u32x4 okv = {pk(kv[0], kv[1]), pk(kv[2], kv[3]), pk(kv[4], kv[5]), pk(kv[6], kv[7])};
        __builtin_amdgcn_raw_buffer_store_b128(okv, sig_rsrc, go, 0, 16);
    }
#define PUBLISH() do { \
        asm volatile("s_waitcnt vmcnt(0)" : "+v"(tag) : "v"(pfv) : "memory"); \
        tag += 1; \
        u32 old_ = 0; \
        if (lane == 0) old_ = __hip_atomic_fetch_add((LAS u32*)(size_t)C_PUB, 1u, __ATOMIC_RELAXED, __HIP_MEMORY_SCOPE_WORKGROUP); \
        old_ = __builtin_amdgcn_readfirstlane(old_); \
        if (old_ == 7 && lane < 2) \
            __hip_atomic_store(lane ? (gu64*)(ws + WS_FLAG2) + w : (gu64*)(ws + WS_FLAG1) + (grp * 32 + ti) * FSTR, tag, __ATOMIC_RELAXED, __HIP_MEMORY_SCOPE_AGENT); \
    } while (0)

    if (wv < 4) {
        asm volatile("" : "+v"(sm[0]), "+v"(sm[1]), "+v"(sm2[0]), "+v"(sm2[1]), "+v"(sme), "+v"(gq[0]), "+v"(gq[1]), "+v"(gwv), "+v"(sd0), "+v"(xt0));
        {
            float z[2];
#pragma unroll
            for (int u = 0; u < 2; ++u) {
                z[u] = dot2(gq[u][0], gwv[0], 0.f) + dot2(gq[u][1], gwv[1], 0.f);
                z[u] += dot2(gq[u][2], gwv[2], 0.f) + dot2(gq[u][3], gwv[3], 0.f);
            }
            auto h = __builtin_amdgcn_permlane32_swap(__float_as_uint(z[0]), __float_as_uint(z[1]), false, false);
            float y = __uint_as_float(h[0]) + __uint_as_float(h[1]);
            y += __uint_as_float(__builtin_amdgcn_update_dpp(0u, __float_as_uint(y), 0x128, 0xf, 0xf, true));
            y += __uint_as_float(__builtin_amdgcn_update_dpp(0u, __float_as_uint(y), 0x124, 0xf, 0xf, true));
            y += __uint_as_float(__builtin_amdgcn_update_dpp(0u, __float_as_uint(y), 0x122, 0xf, 0xf, true));
            y += __uint_as_float(__builtin_amdgcn_update_dpp(0u, __float_as_uint(y), 0x121, 0xf, 0xf, true));
            { auto s = __builtin_amdgcn_permlane16_swap(__float_as_uint(y), __float_as_uint(y), false, false); y = __uint_as_float(s[0]) + __uint_as_float(s[1]); }
            y = rb(y);
            const float g = __builtin_amdgcn_rcpf(1.f + __builtin_amdgcn_exp2f(y * -1.44269504088896341f));
            if ((lane & 31) == 0) __builtin_amdgcn_raw_buffer_store_b32(pk(g, 0.f), g_rsrc, 4 * (8 * w + 2 * wv + (lane >> 5)), 0, 16);
        }
#pragma unroll
        for (int ch = 0; ch < 2; ++ch) {
            const u32 t0 = pk(lo2f(sm[ch][0]) + (ch ? hi2f(sd0) : lo2f(sd0)), 0.f) | (sm[ch][0] & 0xffff0000u);
            if (kj == 0) sm[ch][0] = t0;
        }
        sme[0] = pk(lo2f(sme[0]) + (((lane >> 2) & 1) ? hi2f(sd0) : lo2f(sd0)), 0.f) | (sme[0] & 0xffff0000u);
#pragma unroll
        for (int ch = 0; ch < 2; ++ch) write_copies(L_KTA + ch * KT_CC, kj, sm[ch][0], sm[ch][1], sm[ch][2], sm[ch][3], sm2[ch][0], kj < 255 ? sm2[ch][1] : 0u);
        if (wv == 0) write_edges(L_KTA, lane, sme[0], sme[1]);
        PUBLISH();
        if (wv == 0) {
            __builtin_amdgcn_s_sleep(8);
            wait_flags((const gu64*)(ws + WS_FLAG1) + (grp * 32 + (lane & 31)) * FSTR, tag);
            lds_arrive(C_GO, lane);
        } else {
            if (wv >= 2) {
                lds_await(C_TB, 4);
                ktilde_job(1, wv - 1, lane, xt0, 0.f);
                lds_arrive(C_KB, lane);
            }
            lds_await(C_GO, 1);
        }
        {
            const u32 go = (((wv & 1) * D + d0 + (wv >> 1)) * S) * 2 + lane * 16;
            const u32x4 v0 = __builtin_amdgcn_raw_buffer_load_b128(sig_rsrc, go, 0, 16), v1 = __builtin_amdgcn_raw_buffer_load_b128(sig_rsrc, go + 1024, 0, 16);
            const u32x4 v2 = __builtin_amdgcn_raw_buffer_load_b128(sig_rsrc, go + 2048, 0, 16), v3 = __builtin_amdgcn_raw_buffer_load_b128(sig_rsrc, go + 3072, 0, 16);
            lds_await(C_KB, 6);
            expand_chunk(1, kj, gt0, gt1);
            const int la = L_SIG + wv * SIG_ST + lane * 16;
            lds_st<u32x4>(la, v0); lds_st<u32x4>(la + 1024, v1); lds_st<u32x4>(la + 2048, v2); lds_st<u32x4>(la + 3072, v3);
        }
        lds_arrive(C_KT, lane);
        lds_await(C_KT, 8);
        const int r = lane & 15, q = lane >> 4;
        const int a = (r + 1) & 3;
        const int ga = ((wv & 1) ? L_X : L_KTA) + (wv >> 1) * KT_CC + a * KT_COPY + 2 * (1 + r + 8 * q - a);
        const int fa = L_SIG + wv * SIG_ST + 2 * (240 - 16 * r + 8 * q);
        const int ya = L_Y + wv * 4096 + 2 * (16 * r + 4 * q);
        f32x4 acc[8 - NFIX];
        conv_all(ga, ga + 8, fa, ya, acc);
#pragma unroll
        for (int ct = 0; ct < 8 - NFIX; ++ct) lds_st<u32x2>(ya + 512 * (NFIX + ct), (u32x2){pk(acc[ct][0], acc[ct][1]), pk(acc[ct][2], acc[ct][3])});
        __syncthreads();
    } else {
        asm volatile("" : "+v"(pl), "+v"(pa), "+v"(pb), "+v"(pc), "+v"(pd), "+v"(pe), "+v"(pf), "+v"(dke), "+v"(xt0));
        {
            const float dt = rb(__builtin_amdgcn_exp2f(lo2f(pl) * 1.44269504088896341f));
            const float la = dt * lo2f(pa) * 1.44269504088896341f;
            float rv = dt * lo2f(pb) * 0.15915494309189535f;
            rv -= rintf(rv);
            const u32 tb = L_X + kch * TB_CH + 256 * (kw & 1);
            const cpx w128 = cexp_rev(la * 128.f, rv * 128.f), w1 = cexp_rev(la, rv), u1 = cexp_rev(la * 8.f, rv * -8.f);
            const cpx R0 = cexp_rev(la * 1024.f, __builtin_amdgcn_fractf(rv * 1024.f));
            const float br = dt * lo2f(pc), bi = dt * lo2f(pd), cr = lo2f(pe), ci = lo2f(pf);
            const cpx w256 = cmul(w128, w128), w2 = cmul(w1, w1), u2 = cmul(u1, u1);
            cpx Q[8];
            Q[0] = (cpx){cr * br - ci * bi, -(cr * bi + ci * br)};
            const cpx w3 = cmul(w2, w1), w4 = cmul(w2, w2), u4 = cmul(u2, u2), w384 = cmul(w256, w128), w512 = cmul(w256, w256);
            Q[1] = cmul(Q[0], u1); Q[2] = cmul(Q[0], u2); Q[3] = cmul(Q[1], u2);
            const cpx w5 = cmul(w4, w1), w6 = cmul(w4, w2), w7 = cmul(w4, w3), u8 = cmul(u4, u4);
            st4_addtid<48 * TB_ROW, 49 * TB_ROW, 50 * TB_ROW, 51 * TB_ROW>(tb, pkc(Q[0]), pkc(Q[1]), pkc(Q[2]), pkc(Q[3]));
            st4_addtid<0 * TB_ROW, 1 * TB_ROW, 2 * TB_ROW, 3 * TB_ROW>(tb, 0x3f80u, pkc(w1), pkc(w2), pkc(w3));
#pragma unroll
            for (int b = 0; b < 4; ++b) Q[4 + b] = cmul(Q[b], u4);
            st4_addtid<4 * TB_ROW, 5 * TB_ROW, 6 * TB_ROW, 7 * TB_ROW>(tb, pkc(w4), pkc(w5), pkc(w6), pkc(w7));
            st4_addtid<52 * TB_ROW, 53 * TB_ROW, 54 * TB_ROW, 55 * TB_ROW>(tb, pkc(Q[4]), pkc(Q[5]), pkc(Q[6]), pkc(Q[7]));
            st4_addtid<8 * TB_ROW, 9 * TB_ROW, 10 * TB_ROW, 11 * TB_ROW>(tb, pkc(w128), pkc(cmul(w128, w1)), pkc(cmul(w128, w2)), pkc(cmul(w128, w3)));
            st4_addtid<12 * TB_ROW, 13 * TB_ROW, 14 * TB_ROW, 15 * TB_ROW>(tb, pkc(cmul(w128, w4)), pkc(cmul(w128, w5)), pkc(cmul(w128, w6)), pkc(cmul(w128, w7)));
            st4_addtid<56 * TB_ROW, 57 * TB_ROW, 58 * TB_ROW, 59 * TB_ROW>(tb, pkc(cmul(Q[0], u8)), pkc(cmul(Q[1], u8)), pkc(cmul(Q[2], u8)), pkc(cmul(Q[3], u8)));
            st4_addtid<60 * TB_ROW, 61 * TB_ROW, 62 * TB_ROW, 63 * TB_ROW>(tb, pkc(cmul(Q[4], u8)), pkc(cmul(Q[5], u8)), pkc(cmul(Q[6], u8)), pkc(cmul(Q[7], u8)));
            PUBLISH();
            st4_addtid<16 * TB_ROW, 17 * TB_ROW, 18 * TB_ROW, 19 * TB_ROW>(tb, pkc(w256), pkc(cmul(w256, w2)), pkc(cmul(w256, w4)), pkc(cmul(w256, w6)));
            st4_addtid<20 * TB_ROW, 21 * TB_ROW, 22 * TB_ROW, 23 * TB_ROW>(tb, pkc(w384), pkc(cmul(w384, w2)), pkc(cmul(w384, w4)), pkc(cmul(w384, w6)));
            const cpx z1 = cmul(w512, w128), z2 = cmul(w512, w256), z3 = cmul(w512, w384);
            st4_addtid<24 * TB_ROW, 25 * TB_ROW, 26 * TB_ROW, 27 * TB_ROW>(tb, pkc(w512), pkc(cmul(w512, w4)), pkc(z1), pkc(cmul(z1, w4)));
            st4_addtid<28 * TB_ROW, 29 * TB_ROW, 30 * TB_ROW, 31 * TB_ROW>(tb, pkc(z2), pkc(cmul(z2, w4)), pkc(z3), pkc(cmul(z3, w4)));
            const cpx R1 = cmul(R0, w128), R2 = cmul(R0, w256), R3 = cmul(R1, w256);
            st4_addtid<32 * TB_ROW, 33 * TB_ROW, 34 * TB_ROW, 35 * TB_ROW>(tb, pkc(R0), pkc(R1), pkc(R2), pkc(R3));
            st4_addtid<36 * TB_ROW, 37 * TB_ROW, 38 * TB_ROW, 39 * TB_ROW>(tb, pkc(cmul(R0, w512)), pkc(cmul(R1, w512)), pkc(cmul(R2, w512)), pkc(cmul(R3, w512)));
            lds_st<u32>(tb + 40 * TB_ROW + 4 * lane, pkc(cmul(R0, R0)));
        }
        lds_arrive(C_TB, lane);
        lds_await(C_TB, 4);
        ktilde_job(kw == 3, kw == 3 ? 0 : kw, lane, xt0, ((kw == 0 || kw == 3) && lane == 0) ? lo2f(dke) : 0.f);
        lds_arrive(C_KB, lane);
        lds_await(C_KB, 6);
        expand_chunk(0, kj, gt0, gt1);
        if (kw == 0) { const u32x2 k03 = lds_ld<u32x2>(L_KB + ((lane >> 2) & 1) * 1312); write_edges(L_X, lane, k03[0], k03[1]); }
        lds_arrive(C_KT, lane);
        __builtin_amdgcn_s_setprio(2);
        if (kw == 0) {
            lds_await(C_GO, 1);
            const gu64* f2 = (const gu64*)(ws + WS_FLAG2) + lane;
#pragma unroll 1
            for (u32 spins = 0; spins < (1u << 18); ++spins) {
                const u64 x0 = __hip_atomic_load(f2, __ATOMIC_RELAXED, __HIP_MEMORY_SCOPE_AGENT), x1 = __hip_atomic_load(f2 + 64, __ATOMIC_RELAXED, __HIP_MEMORY_SCOPE_AGENT);
                const u64 x2 = __hip_atomic_load(f2 + 128, __ATOMIC_RELAXED, __HIP_MEMORY_SCOPE_AGENT), x3 = __hip_atomic_load(f2 + 192, __ATOMIC_RELAXED, __HIP_MEMORY_SCOPE_AGENT);
                if (__all(x0 == tag && x1 == tag && x2 == tag && x3 == tag)) break;
                __builtin_amdgcn_s_sleep(12);
            }
            lds_arrive(C_GO2, lane);
            if (w == 0 && lane == 0) __hip_atomic_store((gu64*)(ws + WS_EPOCH), tag, __ATOMIC_RELAXED, __HIP_MEMORY_SCOPE_AGENT);
        }
        lds_await(C_GO2, 1);
        asm volatile("" :: "v"(pfv));
        u32x2 gg[4];
#pragma unroll
        for (int p = 0; p < 4; ++p) gg[p] = __builtin_bit_cast(u32x2, __builtin_amdgcn_raw_buffer_load_b64(g_rsrc, 4 * (512 * p + 2 * kt), 0, 16));
        lds_await(C_KT, 8);
        float cA[2][8], cB[8];
#pragma unroll
        for (int p = 0; p < 4; ++p) {
#pragma unroll
            for (int ch = 0; ch < 2; ++ch) {
                const u32 x = lds_ld<u32>(L_SIG + (2 * ch + 1) * SIG_ST + 2 * (2046 - 512 * p - 2 * kt));
                cA[ch][2 * p] = fmul(lo2f(gg[p][0]), hi2f(x)); cA[ch][2 * p + 1] = fmul(lo2f(gg[p][1]), lo2f(x));
                asm volatile("" : "+v"(cA[ch][2 * p]), "+v"(cA[ch][2 * p + 1]));
            }
            cB[2 * p] = rb(1.f - lo2f(gg[p][0])); cB[2 * p + 1] = rb(1.f - lo2f(gg[p][1]));
            asm volatile("" : "+v"(cB[2 * p]), "+v"(cB[2 * p + 1]));
        }
#pragma unroll
        for (int p = 0; p < 4; ++p) {
            if (p != 1) __syncthreads();
            const int rr = 512 * p + 2 * kt;
            float res[2][2];
#pragma unroll
            for (int ch = 0; ch < 2; ++ch) {
                const u32 ya = lds_ld<u32>(L_Y + (2 * ch) * 4096 + 2 * rr), yb = lds_ld<u32>(L_Y + (2 * ch + 1) * 4096 + 2 * rr);
                res[ch][0] = ffma(cA[ch][2 * p], lo2f(ya), fmul(cB[2 * p], lo2f(yb)));
                res[ch][1] = ffma(cA[ch][2 * p + 1], hi2f(ya), fmul(cB[2 * p + 1], hi2f(yb)));
            }
            *(u32*)(P.out + (size_t)rr * D + d0) = pk(res[0][0], res[1][0]);
            *(u32*)(P.out + (size_t)(rr + 1) * D + d0) = pk(res[0][1], res[1][1]);
        }
    }
    asm volatile(".Lfk_pf_end:");
}

extern "C" void kernel_launch(void* const* d_in, const int* in_sizes, int n_in, void* d_out, int out_size, void* d_ws, size_t ws_size, hipStream_t stream) {
    Params p;
    p.q = (const u16*)d_in[0]; p.k = (const u16*)d_in[1]; p.v = (const u16*)d_in[2]; p.sk = (const u16*)d_in[3];
    p.ssm = (const u16*)d_in[4]; p.sD = (const u16*)d_in[5];
    p.are = (const u16*)d_in[7]; p.aim = (const u16*)d_in[8]; p.bre = (const u16*)d_in[9]; p.bim = (const u16*)d_in[10];
    p.cre = (const u16*)d_in[11]; p.cim = (const u16*)d_in[12]; p.ldt = (const u16*)d_in[13]; p.dsk = (const u16*)d_in[14]; p.gw = (const u16*)d_in[15];
    p.out = (u16*)d_out; p.ws = (char*)d_ws;
    (void)hipFuncSetAttribute((const void*)fused_kernel, hipFuncAttributeMaxDynamicSharedMemorySize, L_END);
    fused_kernel<<<NWG, NT, L_END, stream>>>(p);
}
```
